```python
import torch

def _softmax(x, axis=-1):
    return torch.softmax(x, dim=axis)
def _gelu(x):
    return torch.nn.functional.gelu(x, approximate="tanh")
def _silu(x):
    return torch.nn.functional.silu(x)
def _sigmoid(x):
    return torch.sigmoid(x)
def _layernorm(x, axis=-1, eps=1e-5):
    m = x.mean(dim=axis, keepdim=True)
    v = x.var(dim=axis, keepdim=True, unbiased=False)
    return (x - m) / torch.sqrt(v + eps)
def _scatter_add(tgt, idx, src, axis=0):
    return tgt.clone().index_add_(axis, idx, src)
def _segment_sum(src, seg, n):
    out = torch.zeros((n,) + src.shape[1:], dtype=src.dtype)
    return out.index_add_(0, seg, src)
def _pad(x, pw):
    flat = tuple(v for pair in reversed(pw) for v in pair)
    return torch.nn.functional.pad(x, flat)

kernel_name = 'standalone__gqa_block_cached__near_ridge'

def setup_inputs():

    _w = (lambda a: (lambda t: t.to(torch.bfloat16) if t.is_floating_point() else t)(torch.as_tensor(a)))
    B, Sq, D = 8, 144, 512
    Hq, Hkv, Sk, Dh = 32, 32, 1024, 128
    assert Hq % Hkv == 0, (Hq, Hkv)
    return {
        "x": _w((lambda *s: torch.randn(*s, dtype=torch.bfloat16) * 0.8)(B, Sq, D)),
        "k_cache": _w((lambda *s: torch.randn(*s, dtype=torch.bfloat16) * 0.8)(B, Hkv, Sk, Dh)),
        "v_cache": _w((lambda *s: torch.randn(*s, dtype=torch.bfloat16) * 0.05)(B, Hkv, Sk, Dh)),
        "wq": _w((lambda *s: torch.randn(*s, dtype=torch.bfloat16) * 0.8)(D, Hq * Dh) * (D ** -0.5)),
        "wo": _w((lambda *s: torch.randn(*s, dtype=torch.bfloat16) * 0.05)(Hq * Dh, D) * ((Hq * Dh) ** -0.5)),
    }

def reference(x, k_cache, v_cache, wq, wo):
    B, Sq, D = x.shape
    Hq, Dh = 32, 128
    Hkv = k_cache.shape[1]
    rep = Hq // Hkv
    q = (lambda x, *d: x.permute(*d))((x @ wq).reshape(B, Sq, Hq, Dh), 0, 2, 1, 3)
    k = (lambda x, n, axis: torch.repeat_interleave(x, n, dim=axis))(k_cache, rep, axis=1)
    v = (lambda x, n, axis: torch.repeat_interleave(x, n, dim=axis))(v_cache, rep, axis=1)
    scores = torch.einsum("bhqd,bhkd->bhqk", q, k) * (Dh ** -0.5)
    p = _softmax(scores, axis=-1)
    o = torch.einsum("bhqk,bhkd->bhqd", p, v)
    return (lambda x, *d: x.permute(*d))(o, 0, 2, 1, 3).reshape(B, Sq, Hq * Dh) @ wo
```

```cpp
#include <hip/hip_runtime.h>
#include <hip/hip_bf16.h>
#include <stdint.h>

typedef __attribute__((ext_vector_type(8))) short bf16x8;
typedef __attribute__((ext_vector_type(4))) short s16x4;
typedef __attribute__((ext_vector_type(4))) float f32x4;
typedef __attribute__((ext_vector_type(4))) unsigned u32x4;
typedef __attribute__((ext_vector_type(2))) unsigned u32x2;
#define LAS __attribute__((address_space(3)))
#define DI __device__ __forceinline__

constexpr int NB = 8, SQ = 144, DM = 512, NH = 32, SK = 1024, DH = 128;
constexpr int SLOT = 16384;
constexpr int LDS_BYTES = 163840;
constexpr float QSCALE = 0.08838834764831845f * 1.4426950408889634f;
constexpr float THR = 6.0f;

#define MFMA32(a, b, c) __builtin_amdgcn_mfma_f32_16x16x32_bf16((a), (b), (c), 0, 0, 0)
#define MFMA16(a, b, c) __builtin_amdgcn_mfma_f32_16x16x16bf16_1k((a), (b), (c), 0, 0, 0)

typedef __attribute__((ext_vector_type(2))) __bf16 bf16x2_t;
typedef __attribute__((ext_vector_type(2))) float f32x2;
DI unsigned cvtpk(float lo, float hi) { f32x2 v = {lo, hi}; return __builtin_bit_cast(unsigned, __builtin_convertvector(v, bf16x2_t)); }
typedef short v4i16_t __attribute__((ext_vector_type(4)));
DI s16x4 trread(unsigned a) { return __builtin_bit_cast(s16x4, __builtin_amdgcn_ds_read_tr16_b64_v4i16((LAS v4i16_t*)(uintptr_t)a)); }
DI bf16x8 ldsread16(unsigned a) { return *(const LAS bf16x8*)(uintptr_t)a; }
DI bf16x8 cat(s16x4 lo, s16x4 hi) { return __builtin_shufflevector(lo, hi, 0, 1, 2, 3, 4, 5, 6, 7); }
DI float fmax3(float a, float b, float c) { return __builtin_fmaxf(__builtin_fmaxf(a, b), c); }

DI void dma4_nt(const void* sbase, unsigned ldsdst, unsigned o0, unsigned o1, unsigned o2, unsigned o3) {
    unsigned sv;
    asm volatile("s_mov_b32 %0, m0\n\ts_mov_b32 m0, %6\n\ts_nop 0\n\t"
                 "global_load_lds_dwordx4 %1, %5 nt\n\t"
                 "global_load_lds_dwordx4 %2, %5 offset:1024 nt\n\t"
                 "global_load_lds_dwordx4 %3, %5 offset:2048 nt\n\t"
                 "global_load_lds_dwordx4 %4, %5 offset:3072 nt\n\t"
                 "s_mov_b32 m0, %0"
                 : "=&s"(sv) : "v"(o0), "v"(o1), "v"(o2), "v"(o3), "s"(sbase), "s"(ldsdst) : "memory");
}
DI void dma4(const void* sbase, unsigned ldsdst, unsigned o0, unsigned o1, unsigned o2, unsigned o3) {
    unsigned sv;
    asm volatile("s_mov_b32 %0, m0\n\ts_mov_b32 m0, %6\n\ts_nop 0\n\t"
                 "global_load_lds_dwordx4 %1, %5\n\t"
                 "global_load_lds_dwordx4 %2, %5 offset:1024\n\t"
                 "global_load_lds_dwordx4 %3, %5 offset:2048\n\t"
                 "global_load_lds_dwordx4 %4, %5 offset:3072\n\t"
                 "s_mov_b32 m0, %0"
                 : "=&s"(sv) : "v"(o0), "v"(o1), "v"(o2), "v"(o3), "s"(sbase), "s"(ldsdst) : "memory");
}
template <int OFF> DI void gload16(u32x4& d, const void* sbase, unsigned voff) {
    asm volatile("global_load_dwordx4 %0, %1, %2 offset:%3" : "=v"(d) : "v"(voff), "s"(sbase), "i"(OFF) : "memory");
}
#define WAITV(N) asm volatile("s_waitcnt vmcnt(" #N ")" ::: "memory")
#define BAR() asm volatile("s_waitcnt lgkmcnt(0)\n\ts_barrier" ::: "memory")
#define PIN16(a) asm volatile("" : "+v"(a[0]), "+v"(a[1]), "+v"(a[2]), "+v"(a[3]), "+v"(a[4]), "+v"(a[5]), "+v"(a[6]), "+v"(a[7]), \
    "+v"(a[8]), "+v"(a[9]), "+v"(a[10]), "+v"(a[11]), "+v"(a[12]), "+v"(a[13]), "+v"(a[14]), "+v"(a[15]))

DI unsigned sw3(unsigned row) { return (row & 3u) | ((((row >> 2) ^ (row >> 3)) & 1u) << 2); }
DI float xgroup_max(float v) {
    auto r = __builtin_amdgcn_permlane32_swap(__float_as_uint(v), __float_as_uint(v), false, false);
    v = __builtin_fmaxf(__uint_as_float(r[0]), __uint_as_float(r[1]));
    auto r2 = __builtin_amdgcn_permlane16_swap(__float_as_uint(v), __float_as_uint(v), false, false);
    return __builtin_fmaxf(__uint_as_float(r2[0]), __uint_as_float(r2[1]));
}
DI float xgroup_sum(float v) {
    auto r = __builtin_amdgcn_permlane32_swap(__float_as_uint(v), __float_as_uint(v), false, false);
    v = __uint_as_float(r[0]) + __uint_as_float(r[1]);
    auto r2 = __builtin_amdgcn_permlane16_swap(__float_as_uint(v), __float_as_uint(v), false, false);
    return __uint_as_float(r2[0]) + __uint_as_float(r2[1]);
}
DI bf16x8 pack_natural(const f32x4& X, const f32x4& Y, float sc) {
    unsigned x0 = cvtpk(X[0] * sc, X[1] * sc), x1 = cvtpk(X[2] * sc, X[3] * sc);
    unsigned y0 = cvtpk(Y[0] * sc, Y[1] * sc), y1 = cvtpk(Y[2] * sc, Y[3] * sc);
    auto a0 = __builtin_amdgcn_permlane32_swap(x0, y0, false, false);
    auto a1 = __builtin_amdgcn_permlane32_swap(x1, y1, false, false);
    auto b0 = __builtin_amdgcn_permlane16_swap(a0[0], a0[1], false, false);
    auto b1 = __builtin_amdgcn_permlane16_swap(a1[0], a1[1], false, false);
    u32x4 w = {b0[0], b1[0], b0[1], b1[1]};
    return __builtin_bit_cast(bf16x8, w);
}
DI bf16x8 pack_perm(const f32x4& X, const f32x4& Y, float sc) {
    u32x4 w = {cvtpk(X[0] * sc, X[1] * sc), cvtpk(X[2] * sc, X[3] * sc), cvtpk(Y[0] * sc, Y[1] * sc), cvtpk(Y[2] * sc, Y[3] * sc)};
    return __builtin_bit_cast(bf16x8, w);
}

__global__ __launch_bounds__(512, 2) void attn_k(const __hip_bfloat16* __restrict__ x, const __hip_bfloat16* __restrict__ kc,
                                                 const __hip_bfloat16* __restrict__ vc, const __hip_bfloat16* __restrict__ wq,
                                                 u32x4* __restrict__ ofrag) {
    extern __shared__ __attribute__((aligned(16))) char lds[];
    const int tid = threadIdx.x, lane = tid & 63;
    const int wid = __builtin_amdgcn_readfirstlane(tid >> 6);
    const int bid = blockIdx.x, xcd = bid & 7, idx = bid >> 3;
    const int h = 4 * xcd + (idx & 3), b = idx >> 2;
    const unsigned lds0 = (unsigned)(uintptr_t)lds;
    const unsigned r16 = lane & 15, g = lane >> 4, qq = r16 >> 2, p = lane & 3;
    const int w4 = wid & 3;

    u32x4 xo[16], xe[16];
    {
        const char* xb = (const char*)x + (size_t)b * SQ * DM * 2;
        const unsigned vo = (unsigned)((wid * 16 + r16) * DM * 2 + g * 16), ve = (unsigned)((128 + r16) * DM * 2 + g * 16);
#define XL(s) gload16<(s) * 64>(xo[s], xb, vo);
        XL(0) XL(1) XL(2) XL(3) XL(4) XL(5) XL(6) XL(7) XL(8) XL(9) XL(10) XL(11) XL(12) XL(13) XL(14) XL(15)
#undef XL
#define XL(s) gload16<(s) * 64>(xe[s], xb, ve);
        XL(0) XL(1) XL(2) XL(3) XL(4) XL(5) XL(6) XL(7) XL(8) XL(9) XL(10) XL(11) XL(12) XL(13) XL(14) XL(15)
#undef XL
    }
    unsigned offA[4], offB[4];
    const char* kbase = (const char*)kc + ((size_t)(b * NH + h) * SK + 16 * w4) * 256;
    const char* vbase = (const char*)vc + ((size_t)(b * NH + h) * SK + 16 * w4) * 256;
    const char* wbase = (const char*)wq + (size_t)(16 * w4) * 8192 + h * 256;
#pragma unroll
    for (int j = 0; j < 4; ++j) {
        const unsigned rl = 4 * j + g;
        const unsigned chK = r16 ^ rl, chV = r16 ^ (sw3(rl) << 1);
        const unsigned oK = g * 256 + chK * 16, oV = g * 256 + chV * 16;
        const unsigned oW = rl * 8192 + chV * 16 - 1024 * j;
        offA[j] = wid < 4 ? oW : oK; offB[j] = oV;
    }
    const unsigned ldsw = lds0 + w4 * 4096;
    auto issueW = [&](int c) { dma4(wbase + (size_t)c * 64 * 8192, ldsw + (6 + (c & 3)) * SLOT, offA[0], offA[1], offA[2], offA[3]); };
    auto issueK = [&](int t, int slot) { dma4_nt(kbase + (size_t)t * SLOT, ldsw + slot * SLOT, offA[0], offA[1], offA[2], offA[3]); };
    auto issueV = [&](int t, int slot) { dma4_nt(vbase + (size_t)t * SLOT, ldsw + slot * SLOT, offB[0], offB[1], offB[2], offB[3]); };
    if (wid < 4) { issueW(0); issueW(1); issueW(2); issueW(3); }
    else { issueK(0, 0); issueV(0, 1); issueK(1, 2); issueV(1, 3); issueK(2, 4); issueV(2, 5); }

    f32x4 qa[8], qe;
#pragma unroll
    for (int i = 0; i < 8; ++i) qa[i] = f32x4{0.f, 0.f, 0.f, 0.f};
    qe = f32x4{0.f, 0.f, 0.f, 0.f};
    unsigned wad[8], wade;
    {
        const unsigned s0 = qq | ((g & 1) << 2);
#pragma unroll
        for (int tt = 0; tt < 8; ++tt) wad[tt] = lds0 + 6 * SLOT + 256 * (8 * g + qq) + 32 * (tt ^ s0) + 8 * p;
        wade = lds0 + 6 * SLOT + 256 * (8 * g + qq) + 32 * (wid ^ s0) + 8 * p;
    }
    if (wid < 4) { WAITV(12); } else { WAITV(24); }
    PIN16(xo); PIN16(xe);
#pragma unroll
    for (int c = 0; c < 8; ++c) {
        if (c > 0) {
            if (wid < 4) { if (c <= 5) { WAITV(8); } else if (c == 6) { WAITV(4); } else { WAITV(0); } }
        }
        BAR();
        if (c >= 1) {
            if (c + 3 <= 7) { if (wid < 4) issueW(c + 3); }
            else if (wid >= 4) { if (c == 5) issueK(3, 6); else if (c == 6) issueV(3, 7); else if (c == 7) issueK(4, 8); }
        }
        const unsigned so = (c & 3) * SLOT;
#pragma unroll
        for (int sl = 0; sl < 2; ++sl) {
            const int s = 2 * c + sl;
            const bf16x8 bo = __builtin_bit_cast(bf16x8, xo[s]), be = __builtin_bit_cast(bf16x8, xe[s]);
#pragma unroll
            for (int tt = 0; tt < 8; ++tt) {
                const bf16x8 a = cat(trread(wad[tt] + so + sl * 8192), trread(wad[tt ^ 4] + so + sl * 8192 + 1024));
                qa[tt] = MFMA32(a, bo, qa[tt]);
            }
            const bf16x8 ae = cat(trread(wade + so + sl * 8192), trread((wade ^ 128) + so + sl * 8192 + 1024));
            qe = MFMA32(ae, be, qe);
        }
    }
    bf16x8 qf[4], qx[4];
#pragma unroll
    for (int s = 0; s < 4; ++s) qf[s] = pack_natural(qa[2 * s], qa[2 * s + 1], QSCALE);
    BAR();
    {
        u32x2 w = {cvtpk(qe[0] * QSCALE, qe[1] * QSCALE), cvtpk(qe[2] * QSCALE, qe[3] * QSCALE)};
        *(LAS u32x2*)(uintptr_t)(lds0 + 9 * SLOT + r16 * 272 + wid * 32 + g * 8) = w;
    }
    BAR();
#pragma unroll
    for (int s = 0; s < 4; ++s) qx[s] = ldsread16(lds0 + 9 * SLOT + r16 * 272 + s * 64 + g * 16);

    f32x4 o[8], oe[8];
#pragma unroll
    for (int i = 0; i < 8; ++i) { o[i] = f32x4{0.f, 0.f, 0.f, 0.f}; oe[i] = f32x4{0.f, 0.f, 0.f, 0.f}; }
    float m = 0.f, l = 0.f, me = 0.f, le = 0.f;
    unsigned kad[4], vad[8];
#pragma unroll
    for (int s = 0; s < 4; ++s) kad[s] = lds0 + 256 * r16 + 16 * ((4 * s + g) ^ r16);
    {
        const unsigned s0 = qq | (((g ^ (g >> 1)) & 1) << 2);
#pragma unroll
        for (int tt = 0; tt < 8; ++tt) vad[tt] = lds0 + SLOT + 256 * (4 * g + qq) + 32 * (tt ^ s0) + 8 * p;
    }
    unsigned so = 0;
    for (int t = 0; t < 16; ++t) {
        if (wid >= 4) {
            if (t == 0) { WAITV(28); }
            else if (t <= 12) { WAITV(24); }
            else if (t == 13) { WAITV(16); }
            else if (t == 14) { WAITV(8); }
            else { WAITV(0); }
        }
        BAR();
        if (wid >= 4) {
            if (t == 0) issueV(4, 9);
            else if (t + 4 < 16) { const int sl = (so == 0 ? 8 * SLOT : so - 2 * SLOT) / SLOT; issueK(t + 4, sl); issueV(t + 4, sl + 1); }
        }
        f32x4 sacc[4];
        const f32x4 negm = {-m, -m, -m, -m};
#pragma unroll
        for (int i = 0; i < 4; ++i) {
            sacc[i] = negm;
#pragma unroll
            for (int s = 0; s < 4; ++s) sacc[i] = MFMA32(ldsread16(kad[s] + so + i * 4096), qf[s], sacc[i]);
        }
        float mx = fmax3(sacc[0][0], sacc[0][1], sacc[0][2]);
        mx = fmax3(mx, sacc[0][3], sacc[1][0]); mx = fmax3(mx, sacc[1][1], sacc[1][2]); mx = fmax3(mx, sacc[1][3], sacc[2][0]);
        mx = fmax3(mx, sacc[2][1], sacc[2][2]); mx = fmax3(mx, sacc[2][3], sacc[3][0]); mx = fmax3(mx, sacc[3][1], sacc[3][2]);
        mx = __builtin_fmaxf(mx, sacc[3][3]);
        mx = xgroup_max(mx);
        if (t == 0 || __any(mx > THR)) {
            const float dl = t == 0 ? mx : __builtin_fmaxf(mx, 0.f);
            const float al = t == 0 ? 0.f : __builtin_amdgcn_exp2f(-dl);
            m += dl; l *= al;
#pragma unroll
            for (int i = 0; i < 8; ++i) o[i] *= al;
#pragma unroll
            for (int i = 0; i < 4; ++i) sacc[i] -= dl;
        }
#pragma unroll
        for (int i = 0; i < 4; ++i) {
#pragma unroll
            for (int r = 0; r < 4; ++r) { sacc[i][r] = __builtin_amdgcn_exp2f(sacc[i][r]); l += sacc[i][r]; }
        }
        const bf16x8 p0 = pack_perm(sacc[0], sacc[1], 1.f), p1 = pack_perm(sacc[2], sacc[3], 1.f);
#pragma unroll
        for (int tt = 0; tt < 8; ++tt) {
            const bf16x8 v0 = cat(trread(vad[tt] + so), trread(vad[tt] + so + 4096));
            const bf16x8 v1 = cat(trread(vad[tt] + so + 8192), trread(vad[tt] + so + 12288));
            o[tt] = MFMA32(v0, p0, o[tt]);
            o[tt] = MFMA32(v1, p1, o[tt]);
        }
        if (wid < 4) {
            f32x4 se = {-me, -me, -me, -me};
            const unsigned eo = so + w4 * 4096;
#pragma unroll
            for (int s = 0; s < 4; ++s) se = MFMA32(ldsread16(kad[s] + eo), qx[s], se);
            float mxe = __builtin_fmaxf(fmax3(se[0], se[1], se[2]), se[3]);
            mxe = xgroup_max(mxe);
            if (t == 0 || __any(mxe > THR)) {
                const float dl = t == 0 ? mxe : __builtin_fmaxf(mxe, 0.f);
                const float al = t == 0 ? 0.f : __builtin_amdgcn_exp2f(-dl);
                me += dl; le *= al;
#pragma unroll
                for (int i = 0; i < 8; ++i) oe[i] *= al;
                se -= dl;
            }
#pragma unroll
            for (int r = 0; r < 4; ++r) { se[r] = __builtin_amdgcn_exp2f(se[r]); le += se[r]; }
            u32x2 pw = {cvtpk(se[0], se[1]), cvtpk(se[2], se[3])};
            const s16x4 pe = __builtin_bit_cast(s16x4, pw);
#pragma unroll
            for (int tt = 0; tt < 8; ++tt) oe[tt] = MFMA16(trread(vad[tt] + eo), pe, oe[tt]);
        }
        so = (so == 8 * SLOT) ? 0 : so + 2 * SLOT;
    }
    u32x4* of = ofrag + (size_t)(b * NH + h) * 9 * 4 * 64;
    {
        const float rl = 1.0f / xgroup_sum(l);
#pragma unroll
        for (int s = 0; s < 4; ++s) of[(wid * 4 + s) * 64 + lane] = __builtin_bit_cast(u32x4, pack_perm(o[2 * s], o[2 * s + 1], rl));
    }
    BAR();
    if (wid < 4) {
        const float ls = xgroup_sum(le);
#pragma unroll
        for (int tt = 0; tt < 8; ++tt) *(LAS f32x4*)(uintptr_t)(lds0 + w4 * 8192 + tt * 1024 + lane * 16) = oe[tt];
        if (g == 0) { *(LAS float*)(uintptr_t)(lds0 + 32768 + (w4 * 16 + r16) * 8) = me; *(LAS float*)(uintptr_t)(lds0 + 32768 + (w4 * 16 + r16) * 8 + 4) = ls; }
    }
    BAR();
    if (wid >= 4) {
        float mm[4], ll[4];
#pragma unroll
        for (int i = 0; i < 4; ++i) { mm[i] = *(LAS float*)(uintptr_t)(lds0 + 32768 + (i * 16 + r16) * 8); ll[i] = *(LAS float*)(uintptr_t)(lds0 + 32768 + (i * 16 + r16) * 8 + 4); }
        const float mg = __builtin_fmaxf(__builtin_fmaxf(mm[0], mm[1]), __builtin_fmaxf(mm[2], mm[3]));
        float al[4], lt = 0.f;
#pragma unroll
        for (int i = 0; i < 4; ++i) { al[i] = __builtin_amdgcn_exp2f(mm[i] - mg); lt += al[i] * ll[i]; }
        const float rl = 1.0f / lt;
        f32x4 X = {0.f, 0.f, 0.f, 0.f}, Y = {0.f, 0.f, 0.f, 0.f};
#pragma unroll
        for (int i = 0; i < 4; ++i) {
            X += al[i] * *(LAS f32x4*)(uintptr_t)(lds0 + i * 8192 + (2 * w4) * 1024 + lane * 16);
            Y += al[i] * *(LAS f32x4*)(uintptr_t)(lds0 + i * 8192 + (2 * w4 + 1) * 1024 + lane * 16);
        }
        of[(8 * 4 + w4) * 64 + lane] = __builtin_bit_cast(u32x4, pack_perm(X, Y, rl));
    }
}

__global__ __launch_bounds__(512, 2) void oproj_k(const u32x4* __restrict__ ofrag, const __hip_bfloat16* __restrict__ wo, f32x4* __restrict__ part) {
    extern __shared__ __attribute__((aligned(16))) char lds[];
    const int tid = threadIdx.x, lane = tid & 63;
    const int wid = __builtin_amdgcn_readfirstlane(tid >> 6);
    const int bid = blockIdx.x, hg = bid & 7, idx = bid >> 3, nt = idx & 3, b = idx >> 2;
    const unsigned lds0 = (unsigned)(uintptr_t)lds;
    const unsigned r16 = lane & 15, g = lane >> 4, qq = r16 >> 2, p = lane & 3;
    const int w4 = wid & 3;
    {
        unsigned off[4];
#pragma unroll
        for (int j = 0; j < 4; ++j) { const unsigned rl = 4 * j + g; off[j] = rl * 1024 + (r16 ^ (sw3(rl) << 1)) * 16 - 1024 * j; }
        const char* wb = (const char*)wo + (size_t)(512 * hg + 16 * w4) * 1024 + nt * 256;
#pragma unroll
        for (int c = 0; c < 4; ++c) { const int cc = (wid >> 2) * 4 + c; dma4(wb + (size_t)cc * 64 * 1024, lds0 + cc * SLOT + w4 * 4096, off[0], off[1], off[2], off[3]); }
    }
    u32x4 bo[16], be[16];
    {
        const char* ob = (const char*)(ofrag + (size_t)(b * NH + 4 * hg) * 9 * 4 * 64);
#pragma unroll
        for (int hh = 0; hh < 4; ++hh) {
#pragma unroll
            for (int s = 0; s < 4; ++s) {
                bo[hh * 4 + s] = *(const u32x4*)(ob + (size_t)hh * 36864 + (wid * 4 + s) * 1024 + lane * 16);
                be[hh * 4 + s] = *(const u32x4*)(ob + (size_t)hh * 36864 + (8 * 4 + s) * 1024 + lane * 16);
            }
        }
    }
    f32x4 acc[8], ae;
#pragma unroll
    for (int i = 0; i < 8; ++i) acc[i] = f32x4{0.f, 0.f, 0.f, 0.f};
    ae = f32x4{0.f, 0.f, 0.f, 0.f};
    unsigned vad[8], vade;
    {
        const unsigned s0 = qq | (((g ^ (g >> 1)) & 1) << 2);
#pragma unroll
        for (int tt = 0; tt < 8; ++tt) vad[tt] = lds0 + 256 * (4 * g + qq) + 32 * (tt ^ s0) + 8 * p;
        vade = lds0 + 256 * (4 * g + qq) + 32 * (wid ^ s0) + 8 * p;
    }
    WAITV(0);
    BAR();
#pragma unroll
    for (int s = 0; s < 16; ++s) {
        const unsigned so = (s >> 2) * 32768, si = (s & 3) * 8192;
        const bf16x8 b0 = __builtin_bit_cast(bf16x8, bo[s]), b1 = __builtin_bit_cast(bf16x8, be[s]);
#pragma unroll
        for (int tt = 0; tt < 8; ++tt) {
            const bf16x8 a = cat(trread(vad[tt] + so + si), trread(vad[tt] + so + si + 4096));
            acc[tt] = MFMA32(a, b0, acc[tt]);
        }
        const bf16x8 a = cat(trread(vade + so + si), trread(vade + so + si + 4096));
        ae = MFMA32(a, b1, ae);
    }
    f32x4* pp = part + (size_t)((hg * NB + b) * 4 + nt) * 9 * 8 * 64;
#pragma unroll
    for (int tt = 0; tt < 8; ++tt) pp[(wid * 8 + tt) * 64 + lane] = acc[tt];
    pp[(8 * 8 + wid) * 64 + lane] = ae;
}

__global__ __launch_bounds__(256) void reduce_k(const f32x4* __restrict__ part, __hip_bfloat16* __restrict__ out) {
    const int i = blockIdx.x * 256 + threadIdx.x;
    constexpr int PSZ = NB * 4 * 9 * 8 * 64;
    f32x4 s = part[i];
#pragma unroll
    for (int hg = 1; hg < 8; ++hg) s += part[(size_t)hg * PSZ + i];
    const int lane = i & 63, tt = (i >> 6) & 7, rest = i >> 9, qt = rest % 9, r2 = rest / 9, nt = r2 & 3, b = r2 >> 2;
    const int q = qt * 16 + (lane & 15), n = nt * 128 + tt * 16 + (lane >> 4) * 4;
    u32x2 w = {cvtpk(s[0], s[1]), cvtpk(s[2], s[3])};
    *(u32x2*)((char*)out + ((size_t)(b * SQ + q) * DM + n) * 2) = w;
}

extern "C" void kernel_launch(void* const* d_in, const int* in_sizes, int n_in, void* d_out, int out_size, void* d_ws, size_t ws_size,
                              hipStream_t stream) {
    const __hip_bfloat16* x = (const __hip_bfloat16*)d_in[0];
    const __hip_bfloat16* kc = (const __hip_bfloat16*)d_in[1];
    const __hip_bfloat16* vc = (const __hip_bfloat16*)d_in[2];
    const __hip_bfloat16* wq = (const __hip_bfloat16*)d_in[3];
    const __hip_bfloat16* wo = (const __hip_bfloat16*)d_in[4];
    u32x4* ofrag = (u32x4*)d_ws;
    f32x4* part = (f32x4*)((char*)d_ws + (16u << 20));
    (void)hipFuncSetAttribute((const void*)attn_k, hipFuncAttributeMaxDynamicSharedMemorySize, LDS_BYTES);
    (void)hipFuncSetAttribute((const void*)oproj_k, hipFuncAttributeMaxDynamicSharedMemorySize, 131072);
    attn_k<<<256, 512, LDS_BYTES, stream>>>(x, kc, vc, wq, ofrag);
    oproj_k<<<256, 512, 131072, stream>>>(ofrag, wo, part);
    reduce_k<<<576, 256, 0, stream>>>(part, (__hip_bfloat16*)d_out);
}
```

```cpp
#include <hip/hip_runtime.h>
#include <hip/hip_bf16.h>
#include <stdint.h>
#include <type_traits>

typedef __attribute__((ext_vector_type(8))) short bf16x8;
typedef __attribute__((ext_vector_type(4))) short s16x4;
typedef __attribute__((ext_vector_type(4))) float f32x4;
typedef __attribute__((ext_vector_type(4))) unsigned u32x4;
typedef __attribute__((ext_vector_type(2))) unsigned u32x2;
#define LAS __attribute__((address_space(3)))
#define DI __device__ __forceinline__

constexpr int NB = 8, SQ = 144, DM = 512, NH = 32, SK = 1024, DH = 128;
constexpr int SLOT = 16384;
constexpr int LDS_BYTES = 163840;
constexpr float QSCALE = 0.08838834764831845f * 1.4426950408889634f;
constexpr float THR = 6.0f;

#define MFMA32(a, b, c) __builtin_amdgcn_mfma_f32_16x16x32_bf16((a), (b), (c), 0, 0, 0)
#define MFMA16(a, b, c) __builtin_amdgcn_mfma_f32_16x16x16bf16_1k((a), (b), (c), 0, 0, 0)

typedef __attribute__((ext_vector_type(2))) __bf16 bf16x2_t;
typedef __attribute__((ext_vector_type(2))) float f32x2;
DI unsigned cvtpk(float lo, float hi) { f32x2 v = {lo, hi}; return __builtin_bit_cast(unsigned, __builtin_convertvector(v, bf16x2_t)); }
typedef short v4i16_t __attribute__((ext_vector_type(4)));
typedef LAS char* lptr;
DI lptr LP(unsigned a) { return (lptr)(uintptr_t)a; }
DI s16x4 trread(lptr a) { return __builtin_bit_cast(s16x4, __builtin_amdgcn_ds_read_tr16_b64_v4i16((LAS v4i16_t*)a)); }
DI bf16x8 ldsread16(lptr a) { return *(const LAS bf16x8*)a; }
#define SBAR() __builtin_amdgcn_sched_barrier(0)
DI bf16x8 cat(s16x4 lo, s16x4 hi) { return __builtin_shufflevector(lo, hi, 0, 1, 2, 3, 4, 5, 6, 7); }
DI float fmax3(float a, float b, float c) { return __builtin_fmaxf(__builtin_fmaxf(a, b), c); }

DI void dma4_nt(const void* sbase, unsigned ldsdst, unsigned o0, unsigned o1, unsigned o2, unsigned o3) {
    unsigned sv;
    asm volatile("s_mov_b32 %0, m0\n\ts_mov_b32 m0, %6\n\ts_nop 0\n\t"
                 "global_load_lds_dwordx4 %1, %5 nt\n\t"
                 "global_load_lds_dwordx4 %2, %5 offset:1024 nt\n\t"
                 "global_load_lds_dwordx4 %3, %5 offset:2048 nt\n\t"
                 "global_load_lds_dwordx4 %4, %5 offset:3072 nt\n\t"
                 "s_mov_b32 m0, %0"
                 : "=&s"(sv) : "v"(o0), "v"(o1), "v"(o2), "v"(o3), "s"(sbase), "s"(ldsdst) : "memory");
}
DI void dma4(const void* sbase, unsigned ldsdst, unsigned o0, unsigned o1, unsigned o2, unsigned o3) {
    unsigned sv;
    asm volatile("s_mov_b32 %0, m0\n\ts_mov_b32 m0, %6\n\ts_nop 0\n\t"
                 "global_load_lds_dwordx4 %1, %5\n\t"
                 "global_load_lds_dwordx4 %2, %5 offset:1024\n\t"
                 "global_load_lds_dwordx4 %3, %5 offset:2048\n\t"
                 "global_load_lds_dwordx4 %4, %5 offset:3072\n\t"
                 "s_mov_b32 m0, %0"
                 : "=&s"(sv) : "v"(o0), "v"(o1), "v"(o2), "v"(o3), "s"(sbase), "s"(ldsdst) : "memory");
}
template <int OFF> DI void gload16(u32x4& d, const void* sbase, unsigned voff) {
    asm volatile("global_load_dwordx4 %0, %1, %2 offset:%3" : "=v"(d) : "v"(voff), "s"(sbase), "i"(OFF) : "memory");
}
#define WAITV(N) asm volatile("s_waitcnt vmcnt(" #N ")" ::: "memory")
#define BAR() asm volatile("s_waitcnt lgkmcnt(0)\n\ts_barrier" ::: "memory")
#define PIN16(a) asm volatile("" : "+v"(a[0]), "+v"(a[1]), "+v"(a[2]), "+v"(a[3]), "+v"(a[4]), "+v"(a[5]), "+v"(a[6]), "+v"(a[7]), \
    "+v"(a[8]), "+v"(a[9]), "+v"(a[10]), "+v"(a[11]), "+v"(a[12]), "+v"(a[13]), "+v"(a[14]), "+v"(a[15]))

DI unsigned sw3(unsigned row) { return (row & 3u) | ((((row >> 2) ^ (row >> 3)) & 1u) << 2); }
DI float xgroup_max(float v) {
    auto r = __builtin_amdgcn_permlane32_swap(__float_as_uint(v), __float_as_uint(v), false, false);
    v = __builtin_fmaxf(__uint_as_float(r[0]), __uint_as_float(r[1]));
    auto r2 = __builtin_amdgcn_permlane16_swap(__float_as_uint(v), __float_as_uint(v), false, false);
    return __builtin_fmaxf(__uint_as_float(r2[0]), __uint_as_float(r2[1]));
}
DI float xgroup_sum(float v) {
    auto r = __builtin_amdgcn_permlane32_swap(__float_as_uint(v), __float_as_uint(v), false, false);
    v = __uint_as_float(r[0]) + __uint_as_float(r[1]);
    auto r2 = __builtin_amdgcn_permlane16_swap(__float_as_uint(v), __float_as_uint(v), false, false);
    return __uint_as_float(r2[0]) + __uint_as_float(r2[1]);
}
DI bf16x8 pack_natural(const f32x4& X, const f32x4& Y, float sc) {
    unsigned x0 = cvtpk(X[0] * sc, X[1] * sc), x1 = cvtpk(X[2] * sc, X[3] * sc);
    unsigned y0 = cvtpk(Y[0] * sc, Y[1] * sc), y1 = cvtpk(Y[2] * sc, Y[3] * sc);
    auto a0 = __builtin_amdgcn_permlane32_swap(x0, y0, false, false);
    auto a1 = __builtin_amdgcn_permlane32_swap(x1, y1, false, false);
    auto b0 = __builtin_amdgcn_permlane16_swap(a0[0], a0[1], false, false);
    auto b1 = __builtin_amdgcn_permlane16_swap(a1[0], a1[1], false, false);
    u32x4 w = {b0[0], b1[0], b0[1], b1[1]};
    return __builtin_bit_cast(bf16x8, w);
}
DI bf16x8 pack_perm(const f32x4& X, const f32x4& Y, float sc) {
    u32x4 w = {cvtpk(X[0] * sc, X[1] * sc), cvtpk(X[2] * sc, X[3] * sc), cvtpk(Y[0] * sc, Y[1] * sc), cvtpk(Y[2] * sc, Y[3] * sc)};
    return __builtin_bit_cast(bf16x8, w);
}

#define STAMP(i)
template <bool NT> DI void dma2(const void* sbase, unsigned ldsdst, unsigned o0, unsigned o1) {
    unsigned sv;
    if (NT) asm volatile("s_mov_b32 %0, m0\n\ts_mov_b32 m0, %4\n\ts_nop 0\n\t"
                 "global_load_lds_dwordx4 %1, %3 nt\n\tglobal_load_lds_dwordx4 %2, %3 offset:1024 nt\n\ts_mov_b32 m0, %0"
                 : "=&s"(sv) : "v"(o0), "v"(o1), "s"(sbase), "s"(ldsdst) : "memory");
    else asm volatile("s_mov_b32 %0, m0\n\ts_mov_b32 m0, %4\n\ts_nop 0\n\t"
                 "global_load_lds_dwordx4 %1, %3\n\tglobal_load_lds_dwordx4 %2, %3 offset:1024\n\ts_mov_b32 m0, %0"
                 : "=&s"(sv) : "v"(o0), "v"(o1), "s"(sbase), "s"(ldsdst) : "memory");
}
#define PRIO(x) __builtin_amdgcn_s_setprio(x)
DI void dma1_sc1(const void* sbase, unsigned ldsdst, unsigned o) {
    unsigned sv;
    asm volatile("s_mov_b32 %0, m0\n\ts_mov_b32 m0, %3\n\ts_nop 0\n\tglobal_load_lds_dwordx4 %1, %2 sc1\n\ts_mov_b32 m0, %0"
                 : "=&s"(sv) : "v"(o), "s"(sbase), "s"(ldsdst) : "memory");
}
template <int OFF> DI void store16_sc1(const void* sbase, unsigned voff, u32x4 d) {
    asm volatile("global_store_dwordx4 %0, %1, %2 offset:%3 sc1\n\ts_nop 1" :: "v"(voff), "v"(d), "s"(sbase), "i"(OFF) : "memory");
}
template <int OFF> DI void load16_sc1(u32x4& d, const void* sbase, unsigned voff) {
    asm volatile("global_load_dwordx4 %0, %1, %2 offset:%3 sc1" : "=v"(d) : "v"(voff), "s"(sbase), "i"(OFF) : "memory");
}
typedef __attribute__((address_space(1))) unsigned long long gu64;
typedef __attribute__((address_space(1))) unsigned gu32;
constexpr unsigned TAGOFF = 0x40000000u, TAGX = 0x5A5AA5A5u;
DI unsigned long long mkflag(unsigned ticket) { const unsigned t = ticket + TAGOFF; return ((unsigned long long)t << 32) | (t ^ TAGX); }
DI bool flagok(unsigned long long f, unsigned ticket) { const unsigned hi = (unsigned)(f >> 32), lo = (unsigned)f; return lo == (hi ^ TAGX) && (hi - TAGOFF - ticket + 31u) <= 62u; }
DI void pollflags(const unsigned long long* fl, int n, unsigned ticket, int lane) {
    for (int spin = 0; spin < (1 << 22); ++spin) {
        bool ok = true;
        if (lane < n) ok = flagok(__hip_atomic_load((gu64*)(fl + lane), __ATOMIC_RELAXED, __HIP_MEMORY_SCOPE_AGENT), ticket);
        if (__all(ok)) break;
        __builtin_amdgcn_s_sleep(1);
    }
}
#define DM0 1
#define DM1 9
#define XNT false
#define WNT true
#define ODMA dma4_nt
#define QKPRIO 1
#define PVPRIO 0
constexpr int PB = 7 * SLOT, PSZ = 9 * 2048, MISC = PB + 2 * PSZ;
constexpr int RMX = MISC + 64, LSM = MISC + 64 + 4 * 144 * 4;
constexpr float PLIM = 256.0f;
#define PINV(x) asm volatile("" : "+v"(x))
struct AAddr { unsigned kad[4], va0, va1, pw, pr, lds0; unsigned r16, g; };
DI unsigned ringnext(unsigned x, int n) { x += n * SLOT; return x >= 7 * SLOT ? x - 7 * SLOT : x; }
constexpr float PSH = 8.0f;
constexpr int ALP = LSM;

struct QKState { bf16x8 qf[9][4], kf[2][4]; f32x4 negm[9]; };
template <bool FIRST> DI void fixup_qk(QKState& st, const AAddr& ad, const char* kg, const unsigned po, const unsigned fo, const int ks) {
    bf16x8 kt[4];
#pragma unroll
    for (int s = 0; s < 4; ++s) kt[s] = FIRST ? st.kf[0][s] : *(const bf16x8*)(kg + s * 64);
#pragma unroll
    for (int i = 0; i < 9; ++i) {
        f32x4 sc = st.negm[i];
#pragma unroll
        for (int s = 0; s < 4; ++s) sc = MFMA32(kt[s], st.qf[i][s], sc);
        const float mx = xgroup_max(__builtin_fmaxf(fmax3(sc[0], sc[1], sc[2]), sc[3]));
        if (ad.g == 0) *(LAS float*)(uintptr_t)(ad.lds0 + RMX + (ks * 144 + i * 16 + ad.r16) * 4) = mx;
    }
    BAR();
    if (!FIRST && ks == 0) *(LAS unsigned*)(uintptr_t)(ad.lds0 + MISC + fo) = 0u;
#pragma unroll
    for (int i = 0; i < 9; ++i) {
        const unsigned a = ad.lds0 + RMX + (i * 16 + ad.r16) * 4;
        const float m0 = *(LAS float*)(uintptr_t)a, m1 = *(LAS float*)(uintptr_t)(a + 576), m2 = *(LAS float*)(uintptr_t)(a + 1152), m3 = *(LAS float*)(uintptr_t)(a + 1728);
        const float mx = __builtin_fmaxf(fmax3(m0, m1, m2), m3) + PSH;
        const float dl = FIRST ? mx : __builtin_fmaxf(mx, 0.f);
        st.negm[i] -= dl;
        f32x4 sc = st.negm[i];
#pragma unroll
        for (int s = 0; s < 4; ++s) sc = MFMA32(kt[s], st.qf[i][s], sc);
        u32x2 w = {cvtpk(__builtin_amdgcn_exp2f(sc[0]), __builtin_amdgcn_exp2f(sc[1])), cvtpk(__builtin_amdgcn_exp2f(sc[2]), __builtin_amdgcn_exp2f(sc[3]))};
        *(LAS u32x2*)(LP(ad.pw + po) + i * 2048) = w;
    }
    if (!FIRST) BAR();
}
template <int CB> DI void qk_iter(QKState& st, const AAddr& ad, const int it, const char* kg, const unsigned knext, const unsigned pcur, const int ks) {
    const unsigned pprev = PSZ - pcur, fprev = (pprev ? 4u : 0u), fcur = (pcur ? 4u : 0u);
    f32x4 sc[9];
    const unsigned flagv = *(LAS volatile unsigned*)(uintptr_t)(ad.lds0 + MISC + fprev);
#pragma unroll
    for (int s = 0; s < 4; ++s) st.kf[CB ^ 1][s] = ldsread16(LP(ad.kad[s] + knext));
    sc[0] = st.negm[0];
#pragma unroll
    for (int s = 0; s < 4; ++s) sc[0] = MFMA32(st.kf[CB][s], st.qf[0][s], sc[0]);
    SBAR();
    if (__builtin_expect(__builtin_amdgcn_readfirstlane(flagv) != 0, 0)) {
        fixup_qk<false>(st, ad, kg + (size_t)(it - 1) * SLOT, pprev, fprev, ks);
        sc[0] = st.negm[0];
#pragma unroll
        for (int s = 0; s < 4; ++s) sc[0] = MFMA32(st.kf[CB][s], st.qf[0][s], sc[0]);
    }
    const lptr pwp = LP(ad.pw + pcur);
    f32x4 e; u32x2 w; unsigned gor = 0u;
    SBAR();
#pragma unroll
    for (int j = 4; j < 42; ++j) {
        if (j < 36) { const int i = j >> 2, s = j & 3; sc[i] = MFMA32(st.kf[CB][s], st.qf[i][s], s == 0 ? st.negm[i] : sc[i]); }
        { const int rel = j - 5; if (rel >= 0 && rel < 36) { const int i = rel >> 2, ph = rel & 3; e[ph] = __builtin_amdgcn_exp2f(sc[i][ph]); PINV(e[ph]);
              if (ph == 2) { w[0] = cvtpk(e[0], e[1]); PINV(w[0]); } } }
        { const int rel = j - 9; if (rel >= 0 && (rel & 3) == 0 && rel < 36) { const int i = rel >> 2; (void)i; } }
        if (j >= 8 && ((j - 8) & 3) == 0 && j - 8 < 36) {
            const int i = (j - 8) >> 2;
            w[1] = cvtpk(e[2], e[3]);
            *(LAS u32x2*)(pwp + i * 2048) = w; gor |= w[0] | w[1]; PINV(gor);
        }
        SBAR();
    }
    if (__builtin_expect(__any((gor & 0x40004000u) != 0u), 0)) *(LAS volatile unsigned*)(uintptr_t)(ad.lds0 + MISC + fcur) = 1u;
}

struct PVState { f32x4 o[9][2], ls[3]; };
template <int DG, typename F> DI void pv_iter(PVState& st, const AAddr& ad, const unsigned vo, const unsigned pcur, F&& dma) {
    const unsigned pprev = PSZ - pcur, fprev = (pprev ? 4u : 0u);
    s16x4 vf[2][2][2];
    bf16x8 pf[18];
    const unsigned flagv = *(LAS volatile unsigned*)(uintptr_t)(ad.lds0 + MISC + fprev);
    const lptr v0p = LP(ad.va0 + vo), v1p = LP(ad.va1 + vo);
#pragma unroll
    for (int u = 0; u < 2; ++u) {
        vf[u][0][0] = trread(v0p + u * 8192); vf[u][0][1] = trread(v0p + (u * 8192 + 4096));
        vf[u][1][0] = trread(v1p + u * 8192); vf[u][1][1] = trread(v1p + (u * 8192 + 4096));
    }
    SBAR();
    if (__builtin_expect(__builtin_amdgcn_readfirstlane(flagv) != 0, 0)) {
        BAR();
#pragma unroll
        for (int i = 0; i < 9; ++i) {
            const unsigned a = ad.lds0 + RMX + (i * 16 + ad.r16) * 4;
            const float m0 = *(LAS float*)(uintptr_t)a, m1 = *(LAS float*)(uintptr_t)(a + 576), m2 = *(LAS float*)(uintptr_t)(a + 1152), m3 = *(LAS float*)(uintptr_t)(a + 1728);
            const float dl = __builtin_fmaxf(__builtin_fmaxf(fmax3(m0, m1, m2), m3) + PSH, 0.f);
            const float al = __builtin_amdgcn_exp2f(-dl);
            st.o[i][0] *= al; st.o[i][1] *= al;
            if ((i & 3) == DG && i < 8) st.ls[i >> 2] *= al;
            if (i == 8 && DG == 0) st.ls[2] *= al;
        }
        BAR();
    }
    const lptr prp = LP(ad.pr + pprev);
    const u32x4 onesw = {0x3F803F80u, 0x3F803F80u, 0x3F803F80u, 0x3F803F80u};
    const bf16x8 ones = __builtin_bit_cast(bf16x8, onesw);
#pragma unroll
    for (int k = 0; k < 4; ++k) pf[k] = ldsread16(prp + ((k % 9) * 2048 + (k / 9) * 1024));
    if (DM0 < 0) dma(0);
    if (DM1 < 0) dma(1);
    SBAR();
#pragma unroll
    for (int k = 0; k < 18; ++k) {
        const int u = k / 9, i = k % 9;
        st.o[i][0] = MFMA32(cat(vf[u][0][0], vf[u][0][1]), pf[k], st.o[i][0]);
        if (k + 4 < 18) pf[k + 4] = ldsread16(prp + (((k + 4) % 9) * 2048 + ((k + 4) / 9) * 1024));
        SBAR();
        st.o[i][1] = MFMA32(cat(vf[u][1][0], vf[u][1][1]), pf[k], st.o[i][1]);
        if ((i & 3) == DG && i < 8) st.ls[i >> 2] = MFMA32(ones, pf[k], st.ls[i >> 2]);
        if (i == 8 && DG == 0) st.ls[2] = MFMA32(ones, pf[k], st.ls[2]);
        if (k == DM0) dma(0);
        if (k == DM1) dma(1);
        SBAR();
    }
}

constexpr size_t WS_CTR = 0, WS_F1 = 4096, WS_F2 = WS_F1 + 64 * 256, WS_O = 1u << 20, WS_P = 16u << 20;
__global__ __launch_bounds__(512, 2) void attn_k(const __hip_bfloat16* __restrict__ x, const __hip_bfloat16* __restrict__ kc,
                                                 const __hip_bfloat16* __restrict__ vc, const __hip_bfloat16* __restrict__ wq,
                                                 const __hip_bfloat16* __restrict__ wo, __hip_bfloat16* __restrict__ out,
                                                 char* __restrict__ ws, unsigned long long* stamps) {
    extern __shared__ __attribute__((aligned(16))) char lds[];
    u32x4* ofrag = (u32x4*)(ws + WS_O);
    const int tid = threadIdx.x, lane = tid & 63;
    const int wid = __builtin_amdgcn_readfirstlane(tid >> 6);
    const int bid = blockIdx.x, xcd = bid & 7, idx = bid >> 3;
    const int h = 4 * xcd + (idx & 3), b = idx >> 2;
    const unsigned lds0 = (unsigned)(uintptr_t)lds;
    const unsigned r16 = lane & 15, g = lane >> 4, qq = r16 >> 2, p = lane & 3;
    const int w4 = wid & 3;
    STAMP(0);
    unsigned tick = 0;
    if (wid == 0) {
        const unsigned one = lane == 0 ? 1u : 0u, zo = 0u;
        if (lane == 0) asm volatile("global_atomic_add %0, %1, %2, %3 sc0" : "=v"(tick) : "v"(zo), "v"(one), "s"(ws + WS_CTR + b * 256) : "memory");
    }
    const char* xb = (const char*)x + (size_t)b * SQ * DM * 2;
    const unsigned ldsw = lds0 + wid * 2048;
    unsigned offK[4], offV[4], offO[4], offW[2], offX[2], offE[2];
    const int nt = idx & 3;
    const char* kbase = (const char*)kc + ((size_t)(b * NH + h) * SK + 16 * w4) * 256;
    const char* vbase = (const char*)vc + ((size_t)(b * NH + h) * SK + 16 * w4) * 256;
    const char* obase = (const char*)wo + (size_t)(512 * xcd + 16 * w4) * 1024 + nt * 256;
    const char* wbase = (const char*)wq + (size_t)(8 * wid) * 8192 + h * 256;
    const char* xbase = xb + (size_t)(16 * wid) * 1024;
    const unsigned ldsq = lds0 + w4 * 4096;
#pragma unroll
    for (int j = 0; j < 2; ++j) {
        const unsigned rl = 4 * j + g, r15 = 8 * (wid & 1) + rl;
        offW[j] = rl * 8192 + (r16 ^ (sw3(r15) << 1)) * 16 - 1024 * j;
        const unsigned xr = 8 * j + (lane >> 3);
        offX[j] = xr * 1024 + (((lane & 7) ^ ((xr >> 1) & 7)) * 16) - 1024 * j;
        offE[j] = (lane ^ (2 * wid + j)) * 16;
    }
#pragma unroll
    for (int j = 0; j < 4; ++j) {
        const unsigned rl = 4 * j + g, chV = r16 ^ (sw3(rl) << 1);
        offK[j] = g * 256 + (r16 ^ rl) * 16; offV[j] = g * 256 + chV * 16; offO[j] = rl * 1024 + chV * 16 - 1024 * j;
    }
    auto issueO = [&](int c, unsigned so) { ODMA(obase + (size_t)((2 * nt + c) & 7) * 64 * 1024, ldsq + so, offO[0], offO[1], offO[2], offO[3]); };
#define XSL(c) (2 + 2 * (((c) + 1) % 3))
#define WSL(c) ((((c) + 1) % 3) == 2 ? 8 : 3 + 2 * (((c) + 1) % 3))
    auto issueXW = [&](int c) { dma2<XNT>(xbase + c * 128, ldsw + XSL(c) * SLOT, offX[0], offX[1]);
                                dma2<WNT>(wbase + (size_t)c * 64 * 8192, ldsw + WSL(c) * SLOT, offW[0], offW[1]); };
#define TSRC(t) (t)
    auto issueK = [&](int t, unsigned so) { dma4_nt(kbase + (size_t)TSRC(t) * SLOT, ldsq + so, offK[0], offK[1], offK[2], offK[3]); };
    auto issueV = [&](int t, unsigned so) { dma4_nt(vbase + (size_t)TSRC(t) * SLOT, ldsq + so, offV[0], offV[1], offV[2], offV[3]); };
    dma2<XNT>(xb + (size_t)(128 + 2 * wid) * 1024, ldsw + 7 * SLOT, offE[0], offE[1]);
    issueXW(0);
    __builtin_amdgcn_s_barrier();
    issueXW(1);
    if (tid < 2) *(LAS unsigned*)(uintptr_t)(lds0 + MISC + tid * 4) = 0u;

    const unsigned s0w = qq | ((g & 1) << 2);
    const unsigned wrow = lds0 + 256 * (8 * g + qq) + 8 * p;
    const unsigned wa0 = wrow + 32 * ((2 * w4) ^ s0w), wa1 = wrow + 32 * ((2 * w4 + 1) ^ s0w);
    const unsigned xad = lds0 + 128 * r16 + 16 * (g ^ ((r16 >> 1) & 7));
    unsigned xead[4];
#pragma unroll
    for (int s = 0; s < 4; ++s) xead[s] = lds0 + 7 * SLOT + 1024 * r16 + 16 * ((4 * s + g) ^ r16);
    AAddr ad;
    ad.lds0 = lds0; ad.r16 = r16; ad.g = g;
#pragma unroll
    for (int s = 0; s < 4; ++s) ad.kad[s] = lds0 + 4096 * w4 + 256 * r16 + 16 * ((4 * s + g) ^ r16);
    {
        const unsigned s0 = qq | (((g ^ (g >> 1)) & 1) << 2), vrow = lds0 + 256 * (4 * g + qq) + 8 * p;
        ad.va0 = vrow + 32 * ((2 * w4) ^ s0); ad.va1 = vrow + 32 * ((2 * w4 + 1) ^ s0);
    }
    ad.pr = lds0 + PB + lane * 16;
    ad.pw = lds0 + PB + (w4 >> 1) * 1024 + lane * 16 + (w4 & 1) * 8;
    u32x4* of = ofrag + (size_t)(b * NH + h) * 9 * 4 * 64;

    auto body = [&](auto qhc) {
        constexpr int QH = decltype(qhc)::value, NQ = QH ? 4 : 5, Q0 = QH ? 5 : 0;
        asm volatile("; body %0" :: "i"(QH) : "memory");
        {
            f32x4 acc[NQ][2];
#pragma unroll
            for (int i = 0; i < NQ; ++i) { acc[i][0] = f32x4{0.f, 0.f, 0.f, 0.f}; acc[i][1] = f32x4{0.f, 0.f, 0.f, 0.f}; }
            s16x4 al[2][2], ah[2][2]; bf16x8 xb_[2][NQ];
#define RDA(bu, s, j) do { const int o_ = WSL((s) >> 1) * SLOT + ((s) & 1) * 8192; const unsigned a_ = (j) ? wa1 : wa0; \
            al[bu][j] = trread(LP(a_) + o_); ah[bu][j] = trread(LP(a_ ^ 128) + (o_ + 1024)); } while (0)
#define RDB(bu, s, i) do { if (Q0 + (i) < 8) xb_[bu][i] = ldsread16(LP(xad ^ (((s) & 1) * 64)) + (XSL((s) >> 1) * SLOT + (Q0 + (i)) * 2048)); \
            else xb_[bu][i] = ldsread16(LP(xead[(s) & 3]) + ((s) >> 2) * 256); } while (0)
            WAITV(4);
            asm volatile("" : "+v"(tick));
            if (tid == 0) *(LAS unsigned*)(uintptr_t)(lds0 + MISC + 8) = tick;
            STAMP(1);
            BAR();
            RDA(0, 0, 0); RDA(0, 0, 1);
#pragma unroll
            for (int i = 0; i < NQ; ++i) RDB(0, 0, i);
#pragma unroll
            for (int s = 0; s < 16; ++s) {
                const int bu = s & 1;
                if ((s & 1) && s < 15) {
                    const int c = (s + 1) >> 1;
                    if (c <= 5) { WAITV(4); } else if (c == 6) { if (QH) { WAITV(12); } else { WAITV(4); } } else { if (QH) { WAITV(12); } else { WAITV(0); } }
                    BAR();
                }
                if (s == 0) issueXW(2);
                if ((s & 1) && s <= 9) issueXW((s + 5) >> 1);
                if (QH) {
                    if (s == 9) issueK(0, 0);
                    if (s == 10) issueK(1, SLOT);
                    if (s == 11) issueV(0, 2 * SLOT);
                    if (s == 13) issueK(2, 3 * SLOT);
                    if (s == 15) issueV(1, 4 * SLOT);
                }
                SBAR();
#pragma unroll
                for (int i = 0; i < NQ; ++i) {
                    acc[i][0] = MFMA32(cat(al[bu][0], ah[bu][0]), xb_[bu][i], acc[i][0]);
                    if (s < 15) { if (i < 2) RDA(bu ^ 1, s + 1, i); }
                    SBAR();
                    acc[i][1] = MFMA32(cat(al[bu][1], ah[bu][1]), xb_[bu][i], acc[i][1]);
                    if (s < 15) RDB(bu ^ 1, s + 1, i);
                    SBAR();
                }
            }
            STAMP(2);
            BAR();
#pragma unroll
            for (int i = 0; i < NQ; ++i)
                *(LAS bf16x8*)(LP(lds0 + PB + w4 * 1024 + lane * 16) + (Q0 + i) * 4096) = pack_natural(acc[i][0], acc[i][1], QSCALE);
            if (QH) { WAITV(16); }
            BAR();
        }
        STAMP(3);
        const char* kg = (const char*)kc + ((size_t)(b * NH + h) * SK + 16 * w4 + r16) * 256 + g * 16;
        unsigned ro = 2 * SLOT;
        unsigned pcur = PSZ;
        if constexpr (QH == 0) {
            QKState st;
#pragma unroll
            for (int i = 0; i < 9; ++i) {
#pragma unroll
                for (int s = 0; s < 4; ++s) st.qf[i][s] = ldsread16(LP(lds0 + PB + lane * 16) + (i * 4096 + s * 1024));
                st.negm[i] = f32x4{0.f, 0.f, 0.f, 0.f};
            }
#pragma unroll
            for (int s = 0; s < 4; ++s) { st.kf[0][s] = ldsread16(LP(ad.kad[s])); st.kf[1][s] = ldsread16(LP(lds0 + g * 16) + s * 64); }
            BAR();
            STAMP(4);
#pragma unroll
            for (int i = 0; i < 9; ++i) {
                f32x4 sc = {PSH, PSH, PSH, PSH};
#pragma unroll
                for (int s = 0; s < 4; ++s) sc = MFMA32(st.kf[1][s], st.qf[i][s], sc);
                st.negm[i] = -sc;
            }
            qk_iter<0>(st, ad, 0, kg, SLOT, 0u, w4);
            PRIO(QKPRIO);
            for (int it = 1; it < 16; it += 2) {
                BAR();
                if (it == 1) STAMP(5);
                qk_iter<1>(st, ad, it, kg, ringnext(ro, 1), pcur, w4);
                ro = ringnext(ro, 2); pcur = PSZ - pcur;
                BAR();
                if (it < 15) { qk_iter<0>(st, ad, it + 1, kg, ringnext(ro, 1), pcur, w4); ro = ringnext(ro, 2); pcur = PSZ - pcur; }
            }
            {
                const unsigned pprev = PSZ - pcur, fprev = (pprev ? 4u : 0u);
                const unsigned flagv = *(LAS volatile unsigned*)(uintptr_t)(lds0 + MISC + fprev);
                if (__builtin_expect(__builtin_amdgcn_readfirstlane(flagv) != 0, 0)) fixup_qk<false>(st, ad, kg + (size_t)15 * SLOT, pprev, fprev, w4);
            }
            PRIO(0);
            STAMP(21);
            BAR();
        } else {
            PVState st;
            const f32x4 z = {0.f, 0.f, 0.f, 0.f};
#pragma unroll
            for (int i = 0; i < 9; ++i) { st.o[i][0] = z; st.o[i][1] = z; }
            st.ls[0] = z; st.ls[1] = z; st.ls[2] = z;
            WAITV(12);
            BAR();
            STAMP(4);
            issueK(3, 5 * SLOT); issueV(2, 6 * SLOT); issueK(4, 0);
            PRIO(PVPRIO);
            auto pvloop = [&](auto dgc) {
                constexpr int DG = decltype(dgc)::value;
                asm volatile("; pv %0" :: "i"(DG) : "memory");
                for (int it = 1; it <= 16; ++it) {
                    if (it == 1) { WAITV(16); } else if (it <= 14) { WAITV(12); } else if (it == 15) { WAITV(16); } else { WAITV(20); }
                    BAR();
                    if (it == 1) STAMP(5);
                    const unsigned vo = it == 16 ? ringnext(ro, 6) : ro;
                    pv_iter<DG>(st, ad, vo, pcur, [&](int which) {
                        if (which == 0) { if (it >= 2 && it <= 12) issueK(it + 3, ringnext(ro, 5)); if (it == 13) issueV(15, ringnext(ro, 5)); if (it >= 14) issueO(2 * it - 27, ringnext(ro, 5)); }
                        else { if (it <= 12) issueV(it + 2, ringnext(ro, 6)); else if (it <= 15) issueO(2 * it - 26, ringnext(ro, 6)); }
                    });
                    ro = ringnext(ro, 2); pcur = PSZ - pcur;
                }
            };
            if (w4 == 0) pvloop(std::integral_constant<int, 0>{}); else if (w4 == 1) pvloop(std::integral_constant<int, 1>{});
            else if (w4 == 2) pvloop(std::integral_constant<int, 2>{}); else pvloop(std::integral_constant<int, 3>{});
            PRIO(0);
            STAMP(21);
            if (g == 0) {
                *(LAS float*)(uintptr_t)(lds0 + LSM + (w4 * 16 + r16) * 4) = st.ls[0][0];
                *(LAS float*)(uintptr_t)(lds0 + LSM + ((w4 + 4) * 16 + r16) * 4) = st.ls[1][0];
                if (w4 == 0) *(LAS float*)(uintptr_t)(lds0 + LSM + (128 + r16) * 4) = st.ls[2][0];
            }
            WAITV(0);
            BAR();
#pragma unroll
            for (int i = 0; i < 9; ++i) {
                const float lt = *(LAS float*)(uintptr_t)(lds0 + LSM + (i * 16 + r16) * 4);
                const bf16x8 fr = pack_perm(st.o[i][0], st.o[i][1], __builtin_amdgcn_rcpf(lt));
                store16_sc1<0>((const char*)of + (i * 4 + w4) * 1024, lane * 16, __builtin_bit_cast(u32x4, fr));
                *(LAS bf16x8*)(LP(lds0 + PB + w4 * 1024 + lane * 16) + i * 4096) = fr;
            }
        }
        const unsigned ticket = *(LAS volatile unsigned*)(uintptr_t)(lds0 + MISC + 8);
        bf16x8 af[16][2];
#pragma unroll
        for (int k = 0; k < 12; ++k) {
            const int c = k >> 1, so = ((4 + c) % 7) * SLOT + (k & 1) * 8192;
            af[k][0] = cat(trread(LP(ad.va0) + so), trread(LP(ad.va0) + (so + 4096)));
            af[k][1] = cat(trread(LP(ad.va1) + so), trread(LP(ad.va1) + (so + 4096)));
        }
        BAR();
        STAMP(22);
        unsigned long long* f1 = (unsigned long long*)(ws + WS_F1 + (b * 8 + xcd) * 256);
        if (QH) {
            WAITV(0);
            if (lane == 0) __hip_atomic_store((gu64*)(f1 + nt * 4 + w4), mkflag(ticket), __ATOMIC_RELAXED, __HIP_MEMORY_SCOPE_AGENT);
            issueO(6, 5 * SLOT); issueO(7, 6 * SLOT);
        }
        f32x4 acc[NQ][2];
#pragma unroll
        for (int i = 0; i < NQ; ++i) { acc[i][0] = f32x4{0.f, 0.f, 0.f, 0.f}; acc[i][1] = f32x4{0.f, 0.f, 0.f, 0.f}; }
        const unsigned bsrc = lds0 + Q0 * 4096 + lane * 16;
        const char* og = (const char*)(ofrag + (size_t)(b * NH + 4 * xcd) * 9 * 4 * 64);
        auto pos = [&](auto pc, const unsigned bo) {
            constexpr int P = decltype(pc)::value;
            bf16x8 bf[2][NQ];
            const lptr bp = LP(bsrc + bo);
#pragma unroll
            for (int i = 0; i < NQ; ++i) bf[0][i] = ldsread16(bp + i * 4096);
#pragma unroll
            for (int sp = 0; sp < 4; ++sp) {
                SBAR();
#pragma unroll
                for (int i = 0; i < NQ; ++i) {
                    acc[i][0] = MFMA32(af[4 * P + sp][0], bf[sp & 1][i], acc[i][0]);
                    if (sp < 3) bf[(sp & 1) ^ 1][i] = ldsread16(bp + (i * 4096 + (sp + 1) * 1024));
                    acc[i][1] = MFMA32(af[4 * P + sp][1], bf[sp & 1][i], acc[i][1]);
                    if (P == 1 && sp == 0) dma1_sc1(og + (size_t)((nt + 3) & 3) * 36864 + (i * 8 + wid) * 1024, lds0 + PB + (i * 8 + wid) * 1024, lane * 16);
                    if (P == 2 && i < 4 && sp == 1) {
                        const int k = 12 + i, so = (5 + (i >> 1)) * SLOT + (i & 1) * 8192;
                        af[k][0] = cat(trread(LP(ad.va0) + so), trread(LP(ad.va0) + (so + 4096)));
                        af[k][1] = cat(trread(LP(ad.va1) + so), trread(LP(ad.va1) + (so + 4096)));
                    }
                    SBAR();
                }
            }
        };
        pos(std::integral_constant<int, 0>{}, PB);
        pollflags(f1, 16, ticket, lane);
        STAMP(23);
#pragma unroll
        for (int r = 0; r < 9; ++r) {
            const int q = r * 8 + wid, second = q >= 36;
            dma1_sc1(og + (size_t)((nt + 1 + second) & 3) * 36864 + (q - 36 * second) * 1024, lds0 + q * 1024, lane * 16);
        }
        WAITV(4);
        BAR();
        pos(std::integral_constant<int, 1>{}, 0u);
        if (QH) { WAITV(4); } else { WAITV(5); }
        BAR();
        pos(std::integral_constant<int, 2>{}, 36864u);
        WAITV(0);
        BAR();
        const char* sl = ws + WS_P + ((((size_t)(b * 4 + nt) * 8 + wid) * 8 + xcd) * 10240);
        {
            bf16x8 bf[2][4];
            const lptr bp = LP(bsrc + PB);
#pragma unroll
            for (int sp = 0; sp < 4; ++sp) bf[0][sp] = ldsread16(bp + sp * 1024);
#pragma unroll
            for (int i = 0; i < NQ; ++i) {
                SBAR();
#pragma unroll
                for (int sp = 0; sp < 4; ++sp) {
                    acc[i][0] = MFMA32(af[12 + sp][0], bf[i & 1][sp], acc[i][0]);
                    if (i + 1 < NQ) bf[(i & 1) ^ 1][sp] = ldsread16(bp + ((i + 1) * 4096 + sp * 1024));
                    acc[i][1] = MFMA32(af[12 + sp][1], bf[i & 1][sp], acc[i][1]);
                    if (i > 0 && sp == 2) {
                        store16_sc1<0>(sl + (i - 1) * 2048, lane * 16, __builtin_bit_cast(u32x4, acc[i - 1][0]));
                        store16_sc1<1024>(sl + (i - 1) * 2048, lane * 16, __builtin_bit_cast(u32x4, acc[i - 1][1]));
                    }
                    SBAR();
                }
            }
        }
        SBAR(); asm volatile("s_nop 15"); SBAR();
        STAMP(24);
        {
            store16_sc1<0>(sl + (NQ - 1) * 2048, lane * 16, __builtin_bit_cast(u32x4, acc[NQ - 1][0]));
            store16_sc1<1024>(sl + (NQ - 1) * 2048, lane * 16, __builtin_bit_cast(u32x4, acc[NQ - 1][1]));
            WAITV(0);
            unsigned long long* f2 = (unsigned long long*)(ws + WS_F2 + ((b * 4 + nt) * 8 + wid) * 64);
            if (lane == 0) __hip_atomic_store((gu64*)(f2 + xcd), mkflag(ticket), __ATOMIC_RELAXED, __HIP_MEMORY_SCOPE_AGENT);
        }
        STAMP(25);
        {
            const int nqr = (xcd >> 2) ? 4 : 5, q0r = (xcd >> 2) ? 5 : 0;
            if (wid < nqr) {
                pollflags((const unsigned long long*)(ws + WS_F2 + ((b * 4 + nt) * 8 + xcd) * 64), 8, ticket, lane);
                const char* sr = ws + WS_P + (((size_t)(b * 4 + nt) * 8 + xcd) * 8) * 10240 + wid * 2048;
                u32x4 pa[8], pb[8];
#pragma unroll
                for (int j = 0; j < 8; ++j) { load16_sc1<0>(pa[j], sr + j * 10240, lane * 16); load16_sc1<1024>(pb[j], sr + j * 10240, lane * 16); }
                asm volatile("s_waitcnt vmcnt(0)" : "+v"(pa[0]), "+v"(pa[1]), "+v"(pa[2]), "+v"(pa[3]), "+v"(pa[4]), "+v"(pa[5]), "+v"(pa[6]), "+v"(pa[7]),
                             "+v"(pb[0]), "+v"(pb[1]), "+v"(pb[2]), "+v"(pb[3]), "+v"(pb[4]), "+v"(pb[5]), "+v"(pb[6]), "+v"(pb[7]) :: "memory");
                f32x4 X = __builtin_bit_cast(f32x4, pa[0]), Y = __builtin_bit_cast(f32x4, pb[0]);
#pragma unroll
                for (int j = 1; j < 8; ++j) { X += __builtin_bit_cast(f32x4, pa[j]); Y += __builtin_bit_cast(f32x4, pb[j]); }
                const bf16x8 r = pack_natural(X, Y, 1.0f);
                *(bf16x8*)((char*)out + ((size_t)(b * SQ + (q0r + wid) * 16 + r16) * DM + nt * 128 + (xcd & 3) * 32 + g * 8) * 2) = r;
            }
        }
        if (tid == 0) __hip_atomic_fetch_add((gu32*)(ws + WS_CTR + b * 256), 3u, __ATOMIC_RELAXED, __HIP_MEMORY_SCOPE_AGENT);
        STAMP(26);
    };
    if (wid < 4) body(std::integral_constant<int, 0>{}); else body(std::integral_constant<int, 1>{});
}

extern "C" void kernel_launch(void* const* d_in, const int* in_sizes, int n_in, void* d_out, int out_size, void* d_ws, size_t ws_size,
                              hipStream_t stream) {
    const __hip_bfloat16* x = (const __hip_bfloat16*)d_in[0];
    const __hip_bfloat16* kc = (const __hip_bfloat16*)d_in[1];
    const __hip_bfloat16* vc = (const __hip_bfloat16*)d_in[2];
    const __hip_bfloat16* wq = (const __hip_bfloat16*)d_in[3];
    const __hip_bfloat16* wo = (const __hip_bfloat16*)d_in[4];
    (void)hipFuncSetAttribute((const void*)attn_k, hipFuncAttributeMaxDynamicSharedMemorySize, LDS_BYTES);
    attn_k<<<256, 512, LDS_BYTES, stream>>>(x, kc, vc, wq, wo, (__hip_bfloat16*)d_out, (char*)d_ws, (unsigned long long*)((char*)d_ws + (128u << 20)));
}
```

```cpp
#include <hip/hip_runtime.h>
#include <hip/hip_bf16.h>
#include <stdint.h>
#include <type_traits>

typedef __attribute__((ext_vector_type(8))) short bf16x8;
typedef __attribute__((ext_vector_type(4))) short s16x4;
typedef __attribute__((ext_vector_type(4))) float f32x4;
typedef __attribute__((ext_vector_type(4))) unsigned u32x4;
typedef __attribute__((ext_vector_type(2))) unsigned u32x2;
#define LAS __attribute__((address_space(3)))
#define DI __device__ __forceinline__

constexpr int NB = 8, SQ = 144, DM = 512, NH = 32, SK = 1024, DH = 128;
constexpr int SLOT = 16384;
constexpr int LDS_BYTES = 163840;
constexpr float QSCALE = 0.08838834764831845f * 1.4426950408889634f;
constexpr float THR = 6.0f;

#define MFMA32(a, b, c) __builtin_amdgcn_mfma_f32_16x16x32_bf16((a), (b), (c), 0, 0, 0)
#define MFMA16(a, b, c) __builtin_amdgcn_mfma_f32_16x16x16bf16_1k((a), (b), (c), 0, 0, 0)

typedef __attribute__((ext_vector_type(2))) __bf16 bf16x2_t;
typedef __attribute__((ext_vector_type(2))) float f32x2;
DI unsigned cvtpk(float lo, float hi) { f32x2 v = {lo, hi}; return __builtin_bit_cast(unsigned, __builtin_convertvector(v, bf16x2_t)); }
typedef short v4i16_t __attribute__((ext_vector_type(4)));
typedef LAS char* lptr;
DI lptr LP(unsigned a) { return (lptr)(uintptr_t)a; }
DI s16x4 trread(lptr a) { return __builtin_bit_cast(s16x4, __builtin_amdgcn_ds_read_tr16_b64_v4i16((LAS v4i16_t*)a)); }
DI bf16x8 ldsread16(lptr a) { return *(const LAS bf16x8*)a; }
#define SBAR() __builtin_amdgcn_sched_barrier(0)
DI bf16x8 cat(s16x4 lo, s16x4 hi) { return __builtin_shufflevector(lo, hi, 0, 1, 2, 3, 4, 5, 6, 7); }
DI float fmax3(float a, float b, float c) { return __builtin_fmaxf(__builtin_fmaxf(a, b), c); }

DI void dma4_nt(const void* sbase, unsigned ldsdst, unsigned o0, unsigned o1, unsigned o2, unsigned o3) {
    unsigned sv;
    asm volatile("s_mov_b32 %0, m0\n\ts_mov_b32 m0, %6\n\ts_nop 0\n\t"
                 "global_load_lds_dwordx4 %1, %5 nt\n\t"
                 "global_load_lds_dwordx4 %2, %5 offset:1024 nt\n\t"
                 "global_load_lds_dwordx4 %3, %5 offset:2048 nt\n\t"
                 "global_load_lds_dwordx4 %4, %5 offset:3072 nt\n\t"
                 "s_mov_b32 m0, %0"
                 : "=&s"(sv) : "v"(o0), "v"(o1), "v"(o2), "v"(o3), "s"(sbase), "s"(ldsdst) : "memory");
}
DI void dma4(const void* sbase, unsigned ldsdst, unsigned o0, unsigned o1, unsigned o2, unsigned o3) {
    unsigned sv;
    asm volatile("s_mov_b32 %0, m0\n\ts_mov_b32 m0, %6\n\ts_nop 0\n\t"
                 "global_load_lds_dwordx4 %1, %5\n\t"
                 "global_load_lds_dwordx4 %2, %5 offset:1024\n\t"
                 "global_load_lds_dwordx4 %3, %5 offset:2048\n\t"
                 "global_load_lds_dwordx4 %4, %5 offset:3072\n\t"
                 "s_mov_b32 m0, %0"
                 : "=&s"(sv) : "v"(o0), "v"(o1), "v"(o2), "v"(o3), "s"(sbase), "s"(ldsdst) : "memory");
}
template <int OFF> DI void gload16(u32x4& d, const void* sbase, unsigned voff) {
    asm volatile("global_load_dwordx4 %0, %1, %2 offset:%3" : "=v"(d) : "v"(voff), "s"(sbase), "i"(OFF) : "memory");
}
#define WAITV(N) asm volatile("s_waitcnt vmcnt(" #N ")" ::: "memory")
#define BAR() asm volatile("s_waitcnt lgkmcnt(0)\n\ts_barrier" ::: "memory")
#define PIN16(a) asm volatile("" : "+v"(a[0]), "+v"(a[1]), "+v"(a[2]), "+v"(a[3]), "+v"(a[4]), "+v"(a[5]), "+v"(a[6]), "+v"(a[7]), \
    "+v"(a[8]), "+v"(a[9]), "+v"(a[10]), "+v"(a[11]), "+v"(a[12]), "+v"(a[13]), "+v"(a[14]), "+v"(a[15]))

DI unsigned sw3(unsigned row) { return (row & 3u) | ((((row >> 2) ^ (row >> 3)) & 1u) << 2); }
DI float xgroup_max(float v) {
    auto r = __builtin_amdgcn_permlane32_swap(__float_as_uint(v), __float_as_uint(v), false, false);
    v = __builtin_fmaxf(__uint_as_float(r[0]), __uint_as_float(r[1]));
    auto r2 = __builtin_amdgcn_permlane16_swap(__float_as_uint(v), __float_as_uint(v), false, false);
    return __builtin_fmaxf(__uint_as_float(r2[0]), __uint_as_float(r2[1]));
}
DI float xgroup_sum(float v) {
    auto r = __builtin_amdgcn_permlane32_swap(__float_as_uint(v), __float_as_uint(v), false, false);
    v = __uint_as_float(r[0]) + __uint_as_float(r[1]);
    auto r2 = __builtin_amdgcn_permlane16_swap(__float_as_uint(v), __float_as_uint(v), false, false);
    return __uint_as_float(r2[0]) + __uint_as_float(r2[1]);
}
DI bf16x8 pack_natural(const f32x4& X, const f32x4& Y, float sc) {
    unsigned x0 = cvtpk(X[0] * sc, X[1] * sc), x1 = cvtpk(X[2] * sc, X[3] * sc);
    unsigned y0 = cvtpk(Y[0] * sc, Y[1] * sc), y1 = cvtpk(Y[2] * sc, Y[3] * sc);
    auto a0 = __builtin_amdgcn_permlane32_swap(x0, y0, false, false);
    auto a1 = __builtin_amdgcn_permlane32_swap(x1, y1, false, false);
    auto b0 = __builtin_amdgcn_permlane16_swap(a0[0], a0[1], false, false);
    auto b1 = __builtin_amdgcn_permlane16_swap(a1[0], a1[1], false, false);
    u32x4 w = {b0[0], b1[0], b0[1], b1[1]};
    return __builtin_bit_cast(bf16x8, w);
}
DI bf16x8 pack_perm(const f32x4& X, const f32x4& Y, float sc) {
    u32x4 w = {cvtpk(X[0] * sc, X[1] * sc), cvtpk(X[2] * sc, X[3] * sc), cvtpk(Y[0] * sc, Y[1] * sc), cvtpk(Y[2] * sc, Y[3] * sc)};
    return __builtin_bit_cast(bf16x8, w);
}

#define STAMP(i)
template <bool NT> DI void dma2(const void* sbase, unsigned ldsdst, unsigned o0, unsigned o1) {
    unsigned sv;
    if (NT) asm volatile("s_mov_b32 %0, m0\n\ts_mov_b32 m0, %4\n\ts_nop 0\n\t"
                 "global_load_lds_dwordx4 %1, %3 nt\n\tglobal_load_lds_dwordx4 %2, %3 offset:1024 nt\n\ts_mov_b32 m0, %0"
                 : "=&s"(sv) : "v"(o0), "v"(o1), "s"(sbase), "s"(ldsdst) : "memory");
    else asm volatile("s_mov_b32 %0, m0\n\ts_mov_b32 m0, %4\n\ts_nop 0\n\t"
                 "global_load_lds_dwordx4 %1, %3\n\tglobal_load_lds_dwordx4 %2, %3 offset:1024\n\ts_mov_b32 m0, %0"
                 : "=&s"(sv) : "v"(o0), "v"(o1), "s"(sbase), "s"(ldsdst) : "memory");
}
#define PRIO(x) __builtin_amdgcn_s_setprio(x)
DI void dma1_sc1(const void* sbase, unsigned ldsdst, unsigned o) {
    unsigned sv;
    asm volatile("s_mov_b32 %0, m0\n\ts_mov_b32 m0, %3\n\ts_nop 0\n\tglobal_load_lds_dwordx4 %1, %2 sc1\n\ts_mov_b32 m0, %0"
                 : "=&s"(sv) : "v"(o), "s"(sbase), "s"(ldsdst) : "memory");
}
template <int OFF> DI void store16_sc1(const void* sbase, unsigned voff, u32x4 d) {
    asm volatile("global_store_dwordx4 %0, %1, %2 offset:%3 sc1\n\ts_nop 1" :: "v"(voff), "v"(d), "s"(sbase), "i"(OFF) : "memory");
}
template <int OFF> DI void load16_sc1(u32x4& d, const void* sbase, unsigned voff) {
    asm volatile("global_load_dwordx4 %0, %1, %2 offset:%3 sc1" : "=v"(d) : "v"(voff), "s"(sbase), "i"(OFF) : "memory");
}
typedef __attribute__((address_space(1))) unsigned long long gu64;
typedef __attribute__((address_space(1))) unsigned gu32;
constexpr unsigned TAGOFF = 0x40000000u, TAGX = 0x5A5AA5A5u;
DI unsigned long long mkflag(unsigned ticket) { const unsigned t = ticket + TAGOFF; return ((unsigned long long)t << 32) | (t ^ TAGX); }
DI bool flagok(unsigned long long f, unsigned ticket) { const unsigned hi = (unsigned)(f >> 32), lo = (unsigned)f; return lo == (hi ^ TAGX) && (hi - TAGOFF - ticket + 31u) <= 62u; }
DI void pollflags(const unsigned long long* fl, int n, unsigned ticket, int lane) {
    for (int spin = 0; spin < (1 << 22); ++spin) {
        bool ok = true;
        if (lane < n) ok = flagok(__hip_atomic_load((gu64*)(fl + lane), __ATOMIC_RELAXED, __HIP_MEMORY_SCOPE_AGENT), ticket);
        if (__all(ok)) break;
        __builtin_amdgcn_s_sleep(1);
    }
}
#define DM0 1
#define DM1 7
#define XNT false
#define WNT false
#define ODMA dma4
#define QKPRIO 1
#define PVPRIO 0
DI unsigned pollmask(const unsigned long long* fl, unsigned ticket, int lane) {
    bool ok = true;
    if (lane < 16) ok = flagok(__hip_atomic_load((gu64*)(fl + lane), __ATOMIC_RELAXED, __HIP_MEMORY_SCOPE_AGENT), ticket);
    const unsigned m = (unsigned)__ballot(ok);
    return ((m & 0xFu) == 0xFu ? 1u : 0u) | ((m & 0xF0u) == 0xF0u ? 2u : 0u) | ((m & 0xF00u) == 0xF00u ? 4u : 0u) | ((m & 0xF000u) == 0xF000u ? 8u : 0u);
}
constexpr int PB = 7 * SLOT, PSZ = 9 * 2048, MISC = PB + 2 * PSZ;
constexpr int RMX = MISC + 64, LSM = MISC + 64 + 4 * 144 * 4;
constexpr float PLIM = 256.0f;
#define PINV(x) asm volatile("" : "+v"(x))
struct AAddr { unsigned kad[4], va0, va1, pw, pr, lds0; unsigned r16, g; };
DI unsigned ringnext(unsigned x, int n) { x += n * SLOT; return x >= 7 * SLOT ? x - 7 * SLOT : x; }
constexpr float PSH = 8.0f;
constexpr int ALP = LSM;

struct QKState { bf16x8 qf[9][4], kf[2][4]; f32x4 negm[9]; };
template <bool FIRST> DI void fixup_qk(QKState& st, const AAddr& ad, const char* kg, const unsigned po, const unsigned fo, const int ks) {
    bf16x8 kt[4];
#pragma unroll
    for (int s = 0; s < 4; ++s) kt[s] = FIRST ? st.kf[0][s] : *(const bf16x8*)(kg + s * 64);
#pragma unroll
    for (int i = 0; i < 9; ++i) {
        f32x4 sc = st.negm[i];
#pragma unroll
        for (int s = 0; s < 4; ++s) sc = MFMA32(kt[s], st.qf[i][s], sc);
        const float mx = xgroup_max(__builtin_fmaxf(fmax3(sc[0], sc[1], sc[2]), sc[3]));
        if (ad.g == 0) *(LAS float*)(uintptr_t)(ad.lds0 + RMX + (ks * 144 + i * 16 + ad.r16) * 4) = mx;
    }
    BAR();
    if (!FIRST && ks == 0) *(LAS unsigned*)(uintptr_t)(ad.lds0 + MISC + fo) = 0u;
#pragma unroll
    for (int i = 0; i < 9; ++i) {
        const unsigned a = ad.lds0 + RMX + (i * 16 + ad.r16) * 4;
        const float m0 = *(LAS float*)(uintptr_t)a, m1 = *(LAS float*)(uintptr_t)(a + 576), m2 = *(LAS float*)(uintptr_t)(a + 1152), m3 = *(LAS float*)(uintptr_t)(a + 1728);
        const float mx = __builtin_fmaxf(fmax3(m0, m1, m2), m3) + PSH;
        const float dl = FIRST ? mx : __builtin_fmaxf(mx, 0.f);
        st.negm[i] -= dl;
        f32x4 sc = st.negm[i];
#pragma unroll
        for (int s = 0; s < 4; ++s) sc = MFMA32(kt[s], st.qf[i][s], sc);
        u32x2 w = {cvtpk(__builtin_amdgcn_exp2f(sc[0]), __builtin_amdgcn_exp2f(sc[1])), cvtpk(__builtin_amdgcn_exp2f(sc[2]), __builtin_amdgcn_exp2f(sc[3]))};
        *(LAS u32x2*)(LP(ad.pw + po) + i * 2048) = w;
    }
    if (!FIRST) BAR();
}
template <int CB> DI void qk_iter(QKState& st, const AAddr& ad, const int it, const char* kg, const unsigned knext, const unsigned pcur, const int ks) {
    const unsigned pprev = PSZ - pcur, fprev = (pprev ? 4u : 0u), fcur = (pcur ? 4u : 0u);
    f32x4 sc[9];
    const unsigned flagv = *(LAS volatile unsigned*)(uintptr_t)(ad.lds0 + MISC + fprev);
#pragma unroll
    for (int s = 0; s < 4; ++s) st.kf[CB ^ 1][s] = ldsread16(LP(ad.kad[s] + knext));
    sc[0] = st.negm[0];
#pragma unroll
    for (int s = 0; s < 4; ++s) sc[0] = MFMA32(st.kf[CB][s], st.qf[0][s], sc[0]);
    SBAR();
    if (__builtin_expect(__builtin_amdgcn_readfirstlane(flagv) != 0, 0)) {
        fixup_qk<false>(st, ad, kg + (size_t)(it - 1) * SLOT, pprev, fprev, ks);
        sc[0] = st.negm[0];
#pragma unroll
        for (int s = 0; s < 4; ++s) sc[0] = MFMA32(st.kf[CB][s], st.qf[0][s], sc[0]);
    }
    const lptr pwp = LP(ad.pw + pcur);
    f32x4 e; u32x2 w; unsigned gor = 0u;
    SBAR();
#pragma unroll
    for (int j = 4; j < 42; ++j) {
        if (j < 36) { const int i = j >> 2, s = j & 3; sc[i] = MFMA32(st.kf[CB][s], st.qf[i][s], s == 0 ? st.negm[i] : sc[i]); }
        { const int rel = j - 5; if (rel >= 0 && rel < 36) { const int i = rel >> 2, ph = rel & 3; e[ph] = __builtin_amdgcn_exp2f(sc[i][ph]); PINV(e[ph]);
              if (ph == 2) { w[0] = cvtpk(e[0], e[1]); PINV(w[0]); } } }
        { const int rel = j - 9; if (rel >= 0 && (rel & 3) == 0 && rel < 36) { const int i = rel >> 2; (void)i; } }
        if (j >= 8 && ((j - 8) & 3) == 0 && j - 8 < 36) {
            const int i = (j - 8) >> 2;
            w[1] = cvtpk(e[2], e[3]);
            *(LAS u32x2*)(pwp + i * 2048) = w; gor |= w[0] | w[1]; PINV(gor);
        }
        SBAR();
    }
    if (__builtin_expect(__any((gor & 0x40004000u) != 0u), 0)) *(LAS volatile unsigned*)(uintptr_t)(ad.lds0 + MISC + fcur) = 1u;
}

#define NDEF 4
struct PVState { f32x4 o[9][2], ls[3]; bf16x8 dv[2], dp[NDEF]; };
template <int DG> DI void pv_step(PVState& st, const int i, const bf16x8 a0, const bf16x8 a1, const bf16x8 pf, const bf16x8 ones) {
    st.o[i][0] = MFMA32(a0, pf, st.o[i][0]);
    st.o[i][1] = MFMA32(a1, pf, st.o[i][1]);
    if ((i & 3) == DG && i < 8) st.ls[i >> 2] = MFMA32(ones, pf, st.ls[i >> 2]);
    if (i == 8 && DG == 0) st.ls[2] = MFMA32(ones, pf, st.ls[2]);
}
template <int DG> DI void pv_flush(PVState& st) {
    const u32x4 onesw = {0x3F803F80u, 0x3F803F80u, 0x3F803F80u, 0x3F803F80u};
    const bf16x8 ones = __builtin_bit_cast(bf16x8, onesw);
#pragma unroll
    for (int d = 0; d < NDEF; ++d) { pv_step<DG>(st, 9 - NDEF + d, st.dv[0], st.dv[1], st.dp[d], ones); SBAR(); }
}
template <int DG, typename F> DI void pv_iter(PVState& st, const AAddr& ad, const unsigned vo, const unsigned pcur, F&& dma) {
    const unsigned pprev = PSZ - pcur, fprev = (pprev ? 4u : 0u);
    s16x4 vf[2][2][2];
    bf16x8 pf[18];
    const unsigned flagv = *(LAS volatile unsigned*)(uintptr_t)(ad.lds0 + MISC + fprev);
    const lptr v0p = LP(ad.va0 + vo), v1p = LP(ad.va1 + vo);
    const lptr prp = LP(ad.pr + pprev);
    vf[0][0][0] = trread(v0p); vf[0][0][1] = trread(v0p + 4096);
    vf[0][1][0] = trread(v1p); vf[0][1][1] = trread(v1p + 4096);
#pragma unroll
    for (int k = 0; k < 4; ++k) pf[k] = ldsread16(prp + k * 2048);
    SBAR();
    pv_flush<DG>(st);
    if (__builtin_expect(__builtin_amdgcn_readfirstlane(flagv) != 0, 0)) {
        BAR();
#pragma unroll
        for (int i = 0; i < 9; ++i) {
            const unsigned a = ad.lds0 + RMX + (i * 16 + ad.r16) * 4;
            const float m0 = *(LAS float*)(uintptr_t)a, m1 = *(LAS float*)(uintptr_t)(a + 576), m2 = *(LAS float*)(uintptr_t)(a + 1152), m3 = *(LAS float*)(uintptr_t)(a + 1728);
            const float dl = __builtin_fmaxf(__builtin_fmaxf(fmax3(m0, m1, m2), m3) + PSH, 0.f);
            const float al = __builtin_amdgcn_exp2f(-dl);
            st.o[i][0] *= al; st.o[i][1] *= al;
            if ((i & 3) == DG && i < 8) st.ls[i >> 2] *= al;
            if (i == 8 && DG == 0) st.ls[2] *= al;
        }
        BAR();
#pragma unroll
        for (int k = 0; k < 4; ++k) pf[k] = ldsread16(prp + k * 2048);
    }
    const u32x4 onesw = {0x3F803F80u, 0x3F803F80u, 0x3F803F80u, 0x3F803F80u};
    const bf16x8 ones = __builtin_bit_cast(bf16x8, onesw);
    SBAR();
#pragma unroll
    for (int k = 0; k < 18 - NDEF; ++k) {
        const int u = k / 9, i = k % 9;
        if (k + 4 < 18) pf[k + 4] = ldsread16(prp + (((k + 4) % 9) * 2048 + ((k + 4) / 9) * 1024));
        if (k == 2) { vf[1][0][0] = trread(v0p + 8192); vf[1][0][1] = trread(v0p + 12288); }
        if (k == 3) { vf[1][1][0] = trread(v1p + 8192); vf[1][1][1] = trread(v1p + 12288); }
        pv_step<DG>(st, i, cat(vf[u][0][0], vf[u][0][1]), cat(vf[u][1][0], vf[u][1][1]), pf[k], ones);
        if (k == DM0) dma(0);
        if (k == DM1) dma(1);
        SBAR();
    }
#pragma unroll
    for (int k = 18 - NDEF; k + 4 < 18; ++k) pf[k + 4] = ldsread16(prp + (((k + 4) % 9) * 2048 + ((k + 4) / 9) * 1024));
    st.dv[0] = cat(vf[1][0][0], vf[1][0][1]); st.dv[1] = cat(vf[1][1][0], vf[1][1][1]);
#pragma unroll
    for (int d = 0; d < NDEF; ++d) st.dp[d] = pf[18 - NDEF + d];
}

constexpr size_t WS_CTR = 0, WS_F1 = 4096, WS_F2 = WS_F1 + 64 * 256, WS_O = 1u << 20, WS_P = 16u << 20;
__global__ __launch_bounds__(512, 2) void attn_k(const __hip_bfloat16* __restrict__ x, const __hip_bfloat16* __restrict__ kc,
                                                 const __hip_bfloat16* __restrict__ vc, const __hip_bfloat16* __restrict__ wq,
                                                 const __hip_bfloat16* __restrict__ wo, __hip_bfloat16* __restrict__ out,
                                                 char* __restrict__ ws, unsigned long long* stamps) {
    extern __shared__ __attribute__((aligned(16))) char lds[];
    u32x4* ofrag = (u32x4*)(ws + WS_O);
    const int tid = threadIdx.x, lane = tid & 63;
    const int wid = __builtin_amdgcn_readfirstlane(tid >> 6);
    const int bid = blockIdx.x, xcd = bid & 7, idx = bid >> 3;
    const int h = 4 * xcd + (idx & 3), b = idx >> 2;
    const unsigned lds0 = (unsigned)(uintptr_t)lds;
    const unsigned r16 = lane & 15, g = lane >> 4, qq = r16 >> 2, p = lane & 3;
    const int w4 = wid & 3;
    STAMP(0);
    unsigned tick = 0;
    if (wid == 0) {
        const unsigned one = lane == 0 ? 1u : 0u, zo = 0u;
        if (lane == 0) asm volatile("global_atomic_add %0, %1, %2, %3 sc0" : "=v"(tick) : "v"(zo), "v"(one), "s"(ws + WS_CTR + b * 256) : "memory");
    }
    const char* xb = (const char*)x + (size_t)b * SQ * DM * 2;
    const unsigned ldsw = lds0 + wid * 2048;
    unsigned offK[4], offV[4], offO[4], offW[2], offX[2], offE[2];
    const int nt = idx & 3;
    const int rot = (b + 2 * nt) & 7;
    const char* kbase = (const char*)kc + ((size_t)(b * NH + h) * SK + 16 * w4) * 256;
    const char* vbase = (const char*)vc + ((size_t)(b * NH + h) * SK + 16 * w4) * 256;
    const char* obase = (const char*)wo + (size_t)(512 * xcd + 16 * w4) * 1024 + nt * 256;
    const char* wbase = (const char*)wq + (size_t)(8 * wid) * 8192 + h * 256;
    const char* xbase = xb + (size_t)(16 * wid) * 1024;
    const unsigned ldsq = lds0 + w4 * 4096;
#pragma unroll
    for (int j = 0; j < 2; ++j) {
        const unsigned rl = 4 * j + g, r15 = 8 * (wid & 1) + rl;
        offW[j] = rl * 8192 + (r16 ^ (sw3(r15) << 1)) * 16 - 1024 * j;
        const unsigned xr = 8 * j + (lane >> 3);
        offX[j] = xr * 1024 + (((lane & 7) ^ ((xr >> 1) & 7)) * 16) - 1024 * j;
        offE[j] = (((lane ^ (2 * wid + j)) + 8 * rot) & 63) * 16;
    }
#pragma unroll
    for (int j = 0; j < 4; ++j) {
        const unsigned rl = 4 * j + g, chV = r16 ^ (sw3(rl) << 1);
        offK[j] = g * 256 + (r16 ^ rl) * 16; offV[j] = g * 256 + chV * 16; offO[j] = rl * 1024 + chV * 16 - 1024 * j;
    }
    auto issueO = [&](int c, unsigned so) { ODMA(obase + (size_t)((2 * nt + c) & 7) * 64 * 1024, ldsq + so, offO[0], offO[1], offO[2], offO[3]); };
#define XSL(c) ((((c) + 1) % 3) == 0 ? 2 : (((c) + 1) % 3) == 1 ? 5 : 4)
#define WSL(c) ((((c) + 1) % 3) == 0 ? 3 : (((c) + 1) % 3) == 1 ? 6 : 8)
#define QXO(qt) ((qt) < 8 ? 5 * SLOT + (qt) * 4096 : MISC + 8192)
    auto issueXW = [&](int c) { const int cr = (c + rot) & 7;
                                dma2<XNT>(xbase + cr * 128, ldsw + XSL(c) * SLOT, offX[0], offX[1]);
                                dma2<WNT>(wbase + (size_t)cr * 64 * 8192, ldsw + WSL(c) * SLOT, offW[0], offW[1]); };
#define TSRC(t) (t)
    auto issueK = [&](int t, unsigned so) { dma4_nt(kbase + (size_t)TSRC(t) * SLOT, ldsq + so, offK[0], offK[1], offK[2], offK[3]); };
    auto issueV = [&](int t, unsigned so) { dma4_nt(vbase + (size_t)TSRC(t) * SLOT, ldsq + so, offV[0], offV[1], offV[2], offV[3]); };
    dma2<XNT>(xb + (size_t)(128 + 2 * wid) * 1024, ldsw + 7 * SLOT, offE[0], offE[1]);
    issueXW(0);
    __builtin_amdgcn_s_barrier();
    issueXW(1);
    if (tid < 2) *(LAS unsigned*)(uintptr_t)(lds0 + MISC + tid * 4) = 0u;

    const unsigned s0w = qq | ((g & 1) << 2);
    const unsigned wrow = lds0 + 256 * (8 * g + qq) + 8 * p;
    const unsigned wa0 = wrow + 32 * ((2 * w4) ^ s0w), wa1 = wrow + 32 * ((2 * w4 + 1) ^ s0w);
    const unsigned xad = lds0 + 128 * r16 + 16 * (g ^ ((r16 >> 1) & 7));
    unsigned xead[4];
#pragma unroll
    for (int s = 0; s < 4; ++s) xead[s] = lds0 + 7 * SLOT + 1024 * r16 + 16 * ((4 * s + g) ^ r16);
    AAddr ad;
    ad.lds0 = lds0; ad.r16 = r16; ad.g = g;
#pragma unroll
    for (int s = 0; s < 4; ++s) ad.kad[s] = lds0 + 4096 * w4 + 256 * r16 + 16 * ((4 * s + g) ^ r16);
    {
        const unsigned s0 = qq | (((g ^ (g >> 1)) & 1) << 2), vrow = lds0 + 256 * (4 * g + qq) + 8 * p;
        ad.va0 = vrow + 32 * ((2 * w4) ^ s0); ad.va1 = vrow + 32 * ((2 * w4 + 1) ^ s0);
    }
    ad.pr = lds0 + PB + lane * 16;
    ad.pw = lds0 + PB + (w4 >> 1) * 1024 + lane * 16 + (w4 & 1) * 8;
    u32x4* of = ofrag + (size_t)(b * NH + h) * 9 * 4 * 64;

    auto body = [&](auto qhc) {
        constexpr int QH = decltype(qhc)::value, NQ = QH ? 4 : 5, Q0 = QH ? 5 : 0;
        asm volatile("; body %0" :: "i"(QH) : "memory");
        {
            f32x4 acc[NQ][2];
#pragma unroll
            for (int i = 0; i < NQ; ++i) { acc[i][0] = f32x4{0.f, 0.f, 0.f, 0.f}; acc[i][1] = f32x4{0.f, 0.f, 0.f, 0.f}; }
            s16x4 al[2][2], ah[2][2]; bf16x8 xb_[2][NQ];
#define RDA(bu, s, j) do { const int o_ = WSL((s) >> 1) * SLOT + ((s) & 1) * 8192; const unsigned a_ = (j) ? wa1 : wa0; \
            al[bu][j] = trread(LP(a_) + o_); ah[bu][j] = trread(LP(a_ ^ 128) + (o_ + 1024)); } while (0)
#define RDB(bu, s, i) do { if (Q0 + (i) < 8) xb_[bu][i] = ldsread16(LP(xad ^ (((s) & 1) * 64)) + (XSL((s) >> 1) * SLOT + (Q0 + (i)) * 2048)); \
            else xb_[bu][i] = ldsread16(LP(xead[(s) & 3]) + ((s) >> 2) * 256); } while (0)
            WAITV(4);
            asm volatile("" : "+v"(tick));
            if (tid == 0) *(LAS unsigned*)(uintptr_t)(lds0 + MISC + 8) = tick;
            STAMP(1);
            BAR();
            RDA(0, 0, 0); RDA(0, 0, 1);
#pragma unroll
            for (int i = 0; i < NQ; ++i) RDB(0, 0, i);
#pragma unroll
            for (int s = 0; s < 16; ++s) {
                const int bu = s & 1;
                if ((s & 1) && s < 15) {
                    const int c = (s + 1) >> 1;
                    if (c <= 6) { WAITV(4); } else { if (QH) { WAITV(8); } else { WAITV(0); } }
                    BAR();
                }
                if (!QH) {
                    if (s == 0) issueXW(2);
                    if ((s & 1) && s <= 9) issueXW((s + 5) >> 1);
                } else {
                    if (!(s & 1) && s >= 2 && s <= 10) issueXW((s + 4) >> 1);
                    if (s == 11) issueK(0, 0);
                    if (s == 12) issueK(1, SLOT);
                    if (s == 13) issueV(0, 2 * SLOT);
                    if (s == 14) issueK(2, 3 * SLOT);
                }
                SBAR();
#pragma unroll
                for (int i = 0; i < NQ; ++i) {
                    acc[i][0] = MFMA32(cat(al[bu][0], ah[bu][0]), xb_[bu][i], acc[i][0]);
                    if (s < 15) { if (i < 2) RDA(bu ^ 1, s + 1, i); }
                    SBAR();
                    acc[i][1] = MFMA32(cat(al[bu][1], ah[bu][1]), xb_[bu][i], acc[i][1]);
                    if (s < 15) RDB(bu ^ 1, s + 1, i);
                    SBAR();
                }
                if (QH && s == 0) issueXW(2);
            }
            STAMP(2);
#pragma unroll
            for (int i = 0; i < NQ; ++i)
                *(LAS bf16x8*)(LP(lds0 + w4 * 1024 + lane * 16) + QXO(Q0 + i)) = pack_natural(acc[i][0], acc[i][1], QSCALE);
            if (QH) { WAITV(12); }
            BAR();
        }
        STAMP(3);
        const char* kg = (const char*)kc + ((size_t)(b * NH + h) * SK + 16 * w4 + r16) * 256 + g * 16;
        unsigned ro = 2 * SLOT;
        unsigned pcur = PSZ;
        if constexpr (QH == 0) {
            QKState st;
#pragma unroll
            for (int i = 0; i < 9; ++i) {
#pragma unroll
                for (int s = 0; s < 4; ++s) st.qf[i][s] = ldsread16(LP(lds0 + lane * 16) + (QXO(i) + s * 1024));
                st.negm[i] = f32x4{0.f, 0.f, 0.f, 0.f};
            }
#pragma unroll
            for (int s = 0; s < 4; ++s) { st.kf[0][s] = ldsread16(LP(ad.kad[s])); st.kf[1][s] = ldsread16(LP(lds0 + g * 16) + s * 64); }
#pragma unroll
            for (int i = 0; i < 9; ++i) {
                f32x4 sc = {PSH, PSH, PSH, PSH};
#pragma unroll
                for (int s = 0; s < 4; ++s) sc = MFMA32(st.kf[1][s], st.qf[i][s], sc);
                st.negm[i] = -sc;
            }
            BAR();
            STAMP(4);
            qk_iter<0>(st, ad, 0, kg, SLOT, 0u, w4);
            PRIO(QKPRIO);
            for (int it = 1; it < 16; it += 2) {
                BAR();
                if (it == 1) STAMP(5);
                qk_iter<1>(st, ad, it, kg, ringnext(ro, 1), pcur, w4);
                ro = ringnext(ro, 2); pcur = PSZ - pcur;
                BAR();
                if (it < 15) { qk_iter<0>(st, ad, it + 1, kg, ringnext(ro, 1), pcur, w4); ro = ringnext(ro, 2); pcur = PSZ - pcur; }
            }
            {
                const unsigned pprev = PSZ - pcur, fprev = (pprev ? 4u : 0u);
                const unsigned flagv = *(LAS volatile unsigned*)(uintptr_t)(lds0 + MISC + fprev);
                if (__builtin_expect(__builtin_amdgcn_readfirstlane(flagv) != 0, 0)) fixup_qk<false>(st, ad, kg + (size_t)15 * SLOT, pprev, fprev, w4);
            }
            PRIO(0);
            STAMP(21);
            BAR();
        } else {
            PVState st;
            const f32x4 z = {0.f, 0.f, 0.f, 0.f};
#pragma unroll
            for (int i = 0; i < 9; ++i) { st.o[i][0] = z; st.o[i][1] = z; }
            st.ls[0] = z; st.ls[1] = z; st.ls[2] = z;
            { const bf16x8 zb = {0, 0, 0, 0, 0, 0, 0, 0}; st.dv[0] = zb; st.dv[1] = zb;
#pragma unroll
              for (int d = 0; d < NDEF; ++d) st.dp[d] = zb; }
            issueV(1, 4 * SLOT);
            WAITV(12);
            BAR();
            STAMP(4);
            issueK(3, 5 * SLOT); issueV(2, 6 * SLOT); issueK(4, 0);
            PRIO(PVPRIO);
            auto pvloop = [&](auto dgc) {
                constexpr int DG = decltype(dgc)::value;
                asm volatile("; pv %0" :: "i"(DG) : "memory");
                for (int it = 1; it <= 16; ++it) {
                    if (it == 1) { WAITV(16); } else if (it <= 14) { WAITV(12); } else if (it == 15) { WAITV(16); } else { WAITV(20); }
                    BAR();
                    if (it == 1) STAMP(5);
                    const unsigned vo = it == 16 ? ringnext(ro, 6) : ro;
                    pv_iter<DG>(st, ad, vo, pcur, [&](int which) {
                        if (which == 0) { if (it >= 2 && it <= 12) issueK(it + 3, ringnext(ro, 5)); if (it == 13) issueV(15, ringnext(ro, 5)); if (it >= 14) issueO(2 * it - 27, ringnext(ro, 5)); }
                        else { if (it <= 12) issueV(it + 2, ringnext(ro, 6)); else if (it <= 15) issueO(2 * it - 26, ringnext(ro, 6)); }
                    });
                    ro = ringnext(ro, 2); pcur = PSZ - pcur;
                }
                pv_flush<DG>(st);
            };
            if (w4 == 0) pvloop(std::integral_constant<int, 0>{}); else if (w4 == 1) pvloop(std::integral_constant<int, 1>{});
            else if (w4 == 2) pvloop(std::integral_constant<int, 2>{}); else pvloop(std::integral_constant<int, 3>{});
            PRIO(0);
            STAMP(21);
            if (g == 0) {
                *(LAS float*)(uintptr_t)(lds0 + LSM + (w4 * 16 + r16) * 4) = st.ls[0][0];
                *(LAS float*)(uintptr_t)(lds0 + LSM + ((w4 + 4) * 16 + r16) * 4) = st.ls[1][0];
                if (w4 == 0) *(LAS float*)(uintptr_t)(lds0 + LSM + (128 + r16) * 4) = st.ls[2][0];
            }
            WAITV(0);
            BAR();
#pragma unroll
            for (int i = 0; i < 9; ++i) {
                const float lt = *(LAS float*)(uintptr_t)(lds0 + LSM + (i * 16 + r16) * 4);
                const bf16x8 fr = pack_perm(st.o[i][0], st.o[i][1], __builtin_amdgcn_rcpf(lt));
                store16_sc1<0>((const char*)of + (i * 4 + w4) * 1024, lane * 16, __builtin_bit_cast(u32x4, fr));
                *(LAS bf16x8*)(LP(lds0 + PB + w4 * 1024 + lane * 16) + i * 4096) = fr;
            }
        }
        const unsigned ticket = *(LAS volatile unsigned*)(uintptr_t)(lds0 + MISC + 8);
        bf16x8 af[16][2];
#pragma unroll
        for (int k = 0; k < 12; ++k) {
            const int c = k >> 1, so = ((4 + c) % 7) * SLOT + (k & 1) * 8192;
            af[k][0] = cat(trread(LP(ad.va0) + so), trread(LP(ad.va0) + (so + 4096)));
            af[k][1] = cat(trread(LP(ad.va1) + so), trread(LP(ad.va1) + (so + 4096)));
        }
        BAR();
        STAMP(22);
        unsigned long long* f1 = (unsigned long long*)(ws + WS_F1 + (b * 8 + xcd) * 256);
        if (QH) {
            WAITV(0);
            if (lane == 0) __hip_atomic_store((gu64*)(f1 + nt * 4 + w4), mkflag(ticket), __ATOMIC_RELAXED, __HIP_MEMORY_SCOPE_AGENT);
        }
        f32x4 acc[NQ][2];
#pragma unroll
        for (int i = 0; i < NQ; ++i) { acc[i][0] = f32x4{0.f, 0.f, 0.f, 0.f}; acc[i][1] = f32x4{0.f, 0.f, 0.f, 0.f}; }
        const unsigned bsrc = lds0 + Q0 * 4096 + lane * 16;
        const char* og = (const char*)(ofrag + (size_t)(b * NH + 4 * xcd) * 9 * 4 * 64);
        auto pos = [&](auto pc, const unsigned bo) {
            constexpr int P = decltype(pc)::value;
            bf16x8 bf[2][NQ];
            const lptr bp = LP(bsrc + bo);
#pragma unroll
            for (int i = 0; i < NQ; ++i) bf[0][i] = ldsread16(bp + i * 4096);
#pragma unroll
            for (int sp = 0; sp < 4; ++sp) {
                SBAR();
#pragma unroll
                for (int i = 0; i < NQ; ++i) {
                    acc[i][0] = MFMA32(af[4 * P + sp][0], bf[sp & 1][i], acc[i][0]);
                    if (sp < 3) bf[(sp & 1) ^ 1][i] = ldsread16(bp + (i * 4096 + (sp + 1) * 1024));
                    acc[i][1] = MFMA32(af[4 * P + sp][1], bf[sp & 1][i], acc[i][1]);
                    SBAR();
                }
            }
        };
        unsigned done = 0;
        int ngrp = 0, sq1 = 0, sq2 = 0, sq3 = 0;
        auto fetch = [&](const unsigned rdy) {
#pragma unroll
            for (int p = 1; p < 4; ++p) {
                const int hh = (nt + p) & 3;
                if (((rdy >> hh) & 1u) && !((done >> p) & 1u)) {
#pragma unroll
                    for (int r = 0; r < NQ; ++r) dma1_sc1(og + (size_t)hh * 36864 + (r * 8 + wid) * 1024, lds0 + (p - 1) * 36864 + (r * 8 + wid) * 1024, lane * 16);
                    done |= 1u << p;
                    if (p == 1) sq1 = ngrp; else if (p == 2) sq2 = ngrp; else sq3 = ngrp;
                    ++ngrp;
                }
            }
        };
        auto need = [&](const int p, const int extra) {
            for (int spin = 0; spin < (1 << 22) && !((done >> p) & 1u); ++spin) { fetch(pollmask(f1, ticket, lane)); if (!((done >> p) & 1u)) __builtin_amdgcn_s_sleep(1); }
            const int younger = ngrp - 1 - (p == 1 ? sq1 : p == 2 ? sq2 : sq3);
            (void)extra;
            if (younger >= 2) { if (QH) { WAITV(8); } else { WAITV(10); } }
            else if (younger == 1) { if (QH) { WAITV(4); } else { WAITV(5); } }
            else { WAITV(0); }
        };
        fetch(pollmask(f1, ticket, lane));
        pos(std::integral_constant<int, 0>{}, PB);
        STAMP(23);
        need(1, 0);
        BAR();
        if (QH) { issueO(6, 7 * SLOT); issueO(7, 8 * SLOT); }
        pos(std::integral_constant<int, 1>{}, 0u);
        need(2, 0);
        BAR();
        pos(std::integral_constant<int, 2>{}, 36864u);
        need(3, 0);
        WAITV(0);
        BAR();
#pragma unroll
        for (int k = 12; k < 16; ++k) {
            const int so = (7 + ((k - 12) >> 1)) * SLOT + (k & 1) * 8192;
            af[k][0] = cat(trread(LP(ad.va0) + so), trread(LP(ad.va0) + (so + 4096)));
            af[k][1] = cat(trread(LP(ad.va1) + so), trread(LP(ad.va1) + (so + 4096)));
        }
        const char* sl = ws + WS_P + ((((size_t)(b * 4 + nt) * 8 + wid) * 8 + xcd) * 10240);
        {
            bf16x8 bf[2][4];
            const lptr bp = LP(bsrc + 73728u);
#pragma unroll
            for (int sp = 0; sp < 4; ++sp) bf[0][sp] = ldsread16(bp + sp * 1024);
#pragma unroll
            for (int i = 0; i < NQ; ++i) {
                SBAR();
#pragma unroll
                for (int sp = 0; sp < 4; ++sp) {
                    acc[i][0] = MFMA32(af[12 + sp][0], bf[i & 1][sp], acc[i][0]);
                    if (i + 1 < NQ) bf[(i & 1) ^ 1][sp] = ldsread16(bp + ((i + 1) * 4096 + sp * 1024));
                    acc[i][1] = MFMA32(af[12 + sp][1], bf[i & 1][sp], acc[i][1]);
                    if (i > 0 && sp == 2) {
                        store16_sc1<0>(sl + (i - 1) * 2048, lane * 16, __builtin_bit_cast(u32x4, acc[i - 1][0]));
                        store16_sc1<1024>(sl + (i - 1) * 2048, lane * 16, __builtin_bit_cast(u32x4, acc[i - 1][1]));
                    }
                    SBAR();
                }
            }
        }
        SBAR(); asm volatile("s_nop 15"); SBAR();
        STAMP(24);
        {
            store16_sc1<0>(sl + (NQ - 1) * 2048, lane * 16, __builtin_bit_cast(u32x4, acc[NQ - 1][0]));
            store16_sc1<1024>(sl + (NQ - 1) * 2048, lane * 16, __builtin_bit_cast(u32x4, acc[NQ - 1][1]));
            WAITV(0);
            unsigned long long* f2 = (unsigned long long*)(ws + WS_F2 + ((b * 4 + nt) * 8 + wid) * 64);
            if (lane == 0) __hip_atomic_store((gu64*)(f2 + xcd), mkflag(ticket), __ATOMIC_RELAXED, __HIP_MEMORY_SCOPE_AGENT);
        }
        STAMP(25);
        {
            const int nqr = (xcd >> 2) ? 4 : 5, q0r = (xcd >> 2) ? 5 : 0;
            if (wid < nqr) {
                pollflags((const unsigned long long*)(ws + WS_F2 + ((b * 4 + nt) * 8 + xcd) * 64), 8, ticket, lane);
                const char* sr = ws + WS_P + (((size_t)(b * 4 + nt) * 8 + xcd) * 8) * 10240 + wid * 2048;
                u32x4 pa[8], pb[8];
#pragma unroll
                for (int j = 0; j < 8; ++j) { load16_sc1<0>(pa[j], sr + j * 10240, lane * 16); load16_sc1<1024>(pb[j], sr + j * 10240, lane * 16); }
                asm volatile("s_waitcnt vmcnt(0)" : "+v"(pa[0]), "+v"(pa[1]), "+v"(pa[2]), "+v"(pa[3]), "+v"(pa[4]), "+v"(pa[5]), "+v"(pa[6]), "+v"(pa[7]),
                             "+v"(pb[0]), "+v"(pb[1]), "+v"(pb[2]), "+v"(pb[3]), "+v"(pb[4]), "+v"(pb[5]), "+v"(pb[6]), "+v"(pb[7]) :: "memory");
                f32x4 X = __builtin_bit_cast(f32x4, pa[0]), Y = __builtin_bit_cast(f32x4, pb[0]);
#pragma unroll
                for (int j = 1; j < 8; ++j) { X += __builtin_bit_cast(f32x4, pa[j]); Y += __builtin_bit_cast(f32x4, pb[j]); }
                const bf16x8 r = pack_natural(X, Y, 1.0f);
                *(bf16x8*)((char*)out + ((size_t)(b * SQ + (q0r + wid) * 16 + r16) * DM + nt * 128 + (xcd & 3) * 32 + g * 8) * 2) = r;
            }
        }
        if (tid == 0) __hip_atomic_fetch_add((gu32*)(ws + WS_CTR + b * 256), 3u, __ATOMIC_RELAXED, __HIP_MEMORY_SCOPE_AGENT);
        STAMP(26);
    };
    if (wid < 4) body(std::integral_constant<int, 0>{}); else body(std::integral_constant<int, 1>{});
}

extern "C" void kernel_launch(void* const* d_in, const int* in_sizes, int n_in, void* d_out, int out_size, void* d_ws, size_t ws_size,
                              hipStream_t stream) {
    const __hip_bfloat16* x = (const __hip_bfloat16*)d_in[0];
    const __hip_bfloat16* kc = (const __hip_bfloat16*)d_in[1];
    const __hip_bfloat16* vc = (const __hip_bfloat16*)d_in[2];
    const __hip_bfloat16* wq = (const __hip_bfloat16*)d_in[3];
    const __hip_bfloat16* wo = (const __hip_bfloat16*)d_in[4];
    (void)hipFuncSetAttribute((const void*)attn_k, hipFuncAttributeMaxDynamicSharedMemorySize, LDS_BYTES);
    attn_k<<<256, 512, LDS_BYTES, stream>>>(x, kc, vc, wq, wo, (__hip_bfloat16*)d_out, (char*)d_ws, (unsigned long long*)((char*)d_ws + (128u << 20)));
}
```

```cpp
#include <hip/hip_runtime.h>
#include <hip/hip_bf16.h>
#include <stdint.h>
#include <type_traits>

typedef __attribute__((ext_vector_type(8))) short bf16x8;
typedef __attribute__((ext_vector_type(4))) short s16x4;
typedef __attribute__((ext_vector_type(4))) float f32x4;
typedef __attribute__((ext_vector_type(4))) unsigned u32x4;
typedef __attribute__((ext_vector_type(2))) unsigned u32x2;
typedef __attribute__((ext_vector_type(2))) __bf16 bf16x2_t;
typedef __attribute__((ext_vector_type(2))) float f32x2;
typedef short v4i16_t __attribute__((ext_vector_type(4)));
#define LAS __attribute__((address_space(3)))
#define DI __device__ __forceinline__
typedef LAS char* lptr;
typedef __attribute__((address_space(1))) unsigned long long gu64;
typedef __attribute__((address_space(1))) unsigned gu32;

constexpr int SQ = 144, DM = 512, NH = 32, SK = 1024;
constexpr int SLOT = 16384;
constexpr int LDS_BYTES = 163840;
constexpr float QSCALE = 0.08838834764831845f * 1.4426950408889634f;
constexpr unsigned CODEWARM = 57344u;
constexpr int PB = 7 * SLOT, PSZ = 9 * 2048, MISC = PB + 2 * PSZ;
constexpr int RMX = MISC + 64, LSM = MISC + 64 + 4 * 144 * 4;
constexpr float PSH = 8.0f;
constexpr int NDEF = 4;
constexpr size_t WS_CTR = 0, WS_F1 = 4096, WS_F2 = WS_F1 + 64 * 512, WS_O = 1u << 20, WS_P = 16u << 20;

#define MFMA32(a, b, c) __builtin_amdgcn_mfma_f32_16x16x32_bf16((a), (b), (c), 0, 0, 0)
#define SBAR() __builtin_amdgcn_sched_barrier(0)
#define WAITV(N) asm volatile("s_waitcnt vmcnt(" #N ")" ::: "memory")
#define BAR() asm volatile("s_waitcnt lgkmcnt(0)\n\ts_barrier" ::: "memory")
#define PINV(x) asm volatile("" : "+v"(x))

DI unsigned cvtpk(float lo, float hi) { f32x2 v = {lo, hi}; return __builtin_bit_cast(unsigned, __builtin_convertvector(v, bf16x2_t)); }
DI lptr LP(unsigned a) { return (lptr)(uintptr_t)a; }
DI s16x4 trread(lptr a) { return __builtin_bit_cast(s16x4, __builtin_amdgcn_ds_read_tr16_b64_v4i16((LAS v4i16_t*)a)); }
DI bf16x8 ldsread16(lptr a) { return *(const LAS bf16x8*)a; }
DI bf16x8 cat(s16x4 lo, s16x4 hi) { return __builtin_shufflevector(lo, hi, 0, 1, 2, 3, 4, 5, 6, 7); }
DI float fmax3(float a, float b, float c) { return __builtin_fmaxf(__builtin_fmaxf(a, b), c); }

DI void dma4_nt(const void* sbase, unsigned ldsdst, unsigned o0, unsigned o1, unsigned o2, unsigned o3) {
    unsigned sv;
    asm volatile("s_mov_b32 %0, m0\n\ts_mov_b32 m0, %6\n\ts_nop 0\n\t"
                 "global_load_lds_dwordx4 %1, %5 nt\n\t"
                 "global_load_lds_dwordx4 %2, %5 offset:1024 nt\n\t"
                 "global_load_lds_dwordx4 %3, %5 offset:2048 nt\n\t"
                 "global_load_lds_dwordx4 %4, %5 offset:3072 nt\n\t"
                 "s_mov_b32 m0, %0"
                 : "=&s"(sv) : "v"(o0), "v"(o1), "v"(o2), "v"(o3), "s"(sbase), "s"(ldsdst) : "memory");
}
DI void dma4(const void* sbase, unsigned ldsdst, unsigned o0, unsigned o1, unsigned o2, unsigned o3) {
    unsigned sv;
    asm volatile("s_mov_b32 %0, m0\n\ts_mov_b32 m0, %6\n\ts_nop 0\n\t"
                 "global_load_lds_dwordx4 %1, %5\n\t"
                 "global_load_lds_dwordx4 %2, %5 offset:1024\n\t"
                 "global_load_lds_dwordx4 %3, %5 offset:2048\n\t"
                 "global_load_lds_dwordx4 %4, %5 offset:3072\n\t"
                 "s_mov_b32 m0, %0"
                 : "=&s"(sv) : "v"(o0), "v"(o1), "v"(o2), "v"(o3), "s"(sbase), "s"(ldsdst) : "memory");
}
DI void dma4_k(const void* sbase, unsigned ldsdst, unsigned o) {
    unsigned sv, t;
    asm volatile("s_mov_b32 %0, m0\n\ts_mov_b32 m0, %4\n\tv_xor_b32 %1, 64, %2\n\t"
                 "global_load_lds_dwordx4 %2, %3 nt\n\t"
                 "global_load_lds_dwordx4 %1, %3 offset:1024 nt\n\t"
                 "v_xor_b32 %1, 0x80, %2\n\ts_nop 0\n\t"
                 "global_load_lds_dwordx4 %1, %3 offset:2048 nt\n\t"
                 "v_xor_b32 %1, 0xc0, %2\n\ts_nop 0\n\t"
                 "global_load_lds_dwordx4 %1, %3 offset:3072 nt\n\t"
                 "s_mov_b32 m0, %0"
                 : "=&s"(sv), "=&v"(t) : "v"(o), "s"(sbase), "s"(ldsdst) : "memory");
}
DI void dma1_sc1(const void* sbase, unsigned ldsdst, unsigned o) {
    unsigned sv;
    asm volatile("s_mov_b32 %0, m0\n\ts_mov_b32 m0, %3\n\ts_nop 0\n\tglobal_load_lds_dwordx4 %1, %2 sc1\n\ts_mov_b32 m0, %0"
                 : "=&s"(sv) : "v"(o), "s"(sbase), "s"(ldsdst) : "memory");
}
template <int OFF> DI void store16_sc1(const void* sbase, unsigned voff, u32x4 d) {
    asm volatile("global_store_dwordx4 %0, %1, %2 offset:%3 sc1\n\ts_nop 1" :: "v"(voff), "v"(d), "s"(sbase), "i"(OFF) : "memory");
}
template <int OFF> DI void load16_sc1(u32x4& d, const void* sbase, unsigned voff) {
    asm volatile("global_load_dwordx4 %0, %1, %2 offset:%3 sc1" : "=v"(d) : "v"(voff), "s"(sbase), "i"(OFF) : "memory");
}

DI unsigned sw3(unsigned row) { return (row & 3u) | ((((row >> 2) ^ (row >> 3)) & 1u) << 2); }
DI float xgroup_max(float v) {
    auto r = __builtin_amdgcn_permlane32_swap(__float_as_uint(v), __float_as_uint(v), false, false);
    v = __builtin_fmaxf(__uint_as_float(r[0]), __uint_as_float(r[1]));
    auto r2 = __builtin_amdgcn_permlane16_swap(__float_as_uint(v), __float_as_uint(v), false, false);
    return __builtin_fmaxf(__uint_as_float(r2[0]), __uint_as_float(r2[1]));
}
DI bf16x8 pack_natural(const f32x4& X, const f32x4& Y, float sc) {
    unsigned x0 = cvtpk(X[0] * sc, X[1] * sc), x1 = cvtpk(X[2] * sc, X[3] * sc);
    unsigned y0 = cvtpk(Y[0] * sc, Y[1] * sc), y1 = cvtpk(Y[2] * sc, Y[3] * sc);
    auto a0 = __builtin_amdgcn_permlane32_swap(x0, y0, false, false);
    auto a1 = __builtin_amdgcn_permlane32_swap(x1, y1, false, false);
    auto b0 = __builtin_amdgcn_permlane16_swap(a0[0], a0[1], false, false);
    auto b1 = __builtin_amdgcn_permlane16_swap(a1[0], a1[1], false, false);
    u32x4 w = {b0[0], b1[0], b0[1], b1[1]};
    return __builtin_bit_cast(bf16x8, w);
}
DI bf16x8 pack_perm(const f32x4& X, const f32x4& Y, float sc) {
    u32x4 w = {cvtpk(X[0] * sc, X[1] * sc), cvtpk(X[2] * sc, X[3] * sc), cvtpk(Y[0] * sc, Y[1] * sc), cvtpk(Y[2] * sc, Y[3] * sc)};
    return __builtin_bit_cast(bf16x8, w);
}

constexpr unsigned TAGOFF = 0x40000000u, TAGX = 0x5A5AA5A5u;
DI unsigned long long mkflag(unsigned ticket) { const unsigned t = ticket + TAGOFF; return ((unsigned long long)t << 32) | (t ^ TAGX); }
DI bool flagok(unsigned long long f, unsigned ticket) { const unsigned hi = (unsigned)(f >> 32), lo = (unsigned)f; return lo == (hi ^ TAGX) && (hi - TAGOFF - ticket + 31u) <= 62u; }
DI void pollflags(const unsigned long long* fl, int n, unsigned ticket, int lane) {
    for (int spin = 0; spin < (1 << 22); ++spin) {
        bool ok = true;
        if (lane < n) ok = flagok(__hip_atomic_load((gu64*)(fl + lane), __ATOMIC_RELAXED, __HIP_MEMORY_SCOPE_AGENT), ticket);
        if (__all(ok)) break;
        __builtin_amdgcn_s_sleep(1);
    }
}
DI unsigned pollmask(const unsigned long long* fl, unsigned ticket, int lane) {
    bool ok = false;
    if (lane < 4) ok = flagok(__hip_atomic_load((gu64*)(fl + lane * 16), __ATOMIC_RELAXED, __HIP_MEMORY_SCOPE_AGENT), ticket);
    return (unsigned)__ballot(ok) & 0xFu;
}

DI void reduce_tile(const char* sr, char* dst, const int lane) {
    u32x4 pa[8];
#pragma unroll
    for (int j = 0; j < 8; ++j) load16_sc1<0>(pa[j], sr + j * 5120, lane * 16);
    asm volatile("s_waitcnt vmcnt(0)" : "+v"(pa[0]), "+v"(pa[1]), "+v"(pa[2]), "+v"(pa[3]), "+v"(pa[4]), "+v"(pa[5]), "+v"(pa[6]), "+v"(pa[7]) :: "memory");
    f32x4 X = {0.f, 0.f, 0.f, 0.f}, Y = {0.f, 0.f, 0.f, 0.f};
#pragma unroll
    for (int j = 0; j < 8; ++j) {
        X[0] += __uint_as_float(pa[j][0] << 16); X[1] += __uint_as_float(pa[j][0] & 0xFFFF0000u);
        X[2] += __uint_as_float(pa[j][1] << 16); X[3] += __uint_as_float(pa[j][1] & 0xFFFF0000u);
        Y[0] += __uint_as_float(pa[j][2] << 16); Y[1] += __uint_as_float(pa[j][2] & 0xFFFF0000u);
        Y[2] += __uint_as_float(pa[j][3] << 16); Y[3] += __uint_as_float(pa[j][3] & 0xFFFF0000u);
    }
    __builtin_nontemporal_store(pack_natural(X, Y, 1.0f), (bf16x8*)dst);
}

struct AAddr { unsigned kad[4], va0, va1, pw, pr, lds0; unsigned r16, g; };
DI unsigned ringnext(unsigned x, int n) { x += n * SLOT; return x >= 7 * SLOT ? x - 7 * SLOT : x; }

struct QKState { bf16x8 qf[9][4], kf[2][4]; f32x4 negm[9]; };
DI void fixup_qk(QKState& st, const AAddr& ad, const char* kg, const unsigned po, const unsigned fo, const int ks) {
    bf16x8 kt[4];
    unsigned lo = ad.r16 * 256 + ad.g * 16;
    PINV(lo);
#pragma unroll
    for (int s = 0; s < 4; ++s) kt[s] = *(const bf16x8*)(kg + lo + s * 64);
#pragma unroll
    for (int i = 0; i < 9; ++i) {
        f32x4 sc = st.negm[i];
#pragma unroll
        for (int s = 0; s < 4; ++s) sc = MFMA32(kt[s], st.qf[i][s], sc);
        const float mx = xgroup_max(__builtin_fmaxf(fmax3(sc[0], sc[1], sc[2]), sc[3]));
        if (ad.g == 0) *(LAS float*)(uintptr_t)(ad.lds0 + RMX + (ks * 144 + i * 16 + ad.r16) * 4) = mx;
    }
    BAR();
    if (ks == 0) *(LAS unsigned*)(uintptr_t)(ad.lds0 + MISC + fo) = 0u;
#pragma unroll
    for (int i = 0; i < 9; ++i) {
        const unsigned a = ad.lds0 + RMX + (i * 16 + ad.r16) * 4;
        const float m0 = *(LAS float*)(uintptr_t)a, m1 = *(LAS float*)(uintptr_t)(a + 576), m2 = *(LAS float*)(uintptr_t)(a + 1152), m3 = *(LAS float*)(uintptr_t)(a + 1728);
        const float dl = __builtin_fmaxf(__builtin_fmaxf(fmax3(m0, m1, m2), m3) + PSH, 0.f);
        st.negm[i] -= dl;
        f32x4 sc = st.negm[i];
#pragma unroll
        for (int s = 0; s < 4; ++s) sc = MFMA32(kt[s], st.qf[i][s], sc);
        u32x2 w = {cvtpk(__builtin_amdgcn_exp2f(sc[0]), __builtin_amdgcn_exp2f(sc[1])), cvtpk(__builtin_amdgcn_exp2f(sc[2]), __builtin_amdgcn_exp2f(sc[3]))};
        *(LAS u32x2*)(LP(ad.pw + po) + i * 2048) = w;
    }
    BAR();
}
template <int CB> DI void qk_iter(QKState& st, const AAddr& ad, const int it, const char* kg, const unsigned knext, const unsigned pcur, const int ks) {
    const unsigned pprev = PSZ - pcur, fprev = (pprev ? 4u : 0u), fcur = (pcur ? 4u : 0u);
    f32x4 sc[9];
    const unsigned flagv = *(LAS volatile unsigned*)(uintptr_t)(ad.lds0 + MISC + fprev);
#pragma unroll
    for (int s = 0; s < 4; ++s) st.kf[CB ^ 1][s] = ldsread16(LP(ad.kad[s] + knext));
    sc[0] = st.negm[0];
#pragma unroll
    for (int s = 0; s < 4; ++s) sc[0] = MFMA32(st.kf[CB][s], st.qf[0][s], sc[0]);
    SBAR();
    if (__builtin_expect(__builtin_amdgcn_readfirstlane(flagv) != 0, 0)) {
        fixup_qk(st, ad, kg + (size_t)(it - 1) * SLOT, pprev, fprev, ks);
        sc[0] = st.negm[0];
#pragma unroll
        for (int s = 0; s < 4; ++s) sc[0] = MFMA32(st.kf[CB][s], st.qf[0][s], sc[0]);
    }
    const lptr pwp = LP(ad.pw + pcur);
    f32x4 e; u32x2 w; unsigned gor = 0u;
    SBAR();
#pragma unroll
    for (int j = 4; j < 42; ++j) {
        if (j < 36) { const int i = j >> 2, s = j & 3; sc[i] = MFMA32(st.kf[CB][s], st.qf[i][s], s == 0 ? st.negm[i] : sc[i]); }
        { const int rel = j - 5; if (rel >= 0 && rel < 36) { const int i = rel >> 2, ph = rel & 3; e[ph] = __builtin_amdgcn_exp2f(sc[i][ph]); PINV(e[ph]);
              if (ph == 2) { w[0] = cvtpk(e[0], e[1]); PINV(w[0]); } } }
        if (j >= 8 && ((j - 8) & 3) == 0 && j - 8 < 36) {
            const int i = (j - 8) >> 2;
            w[1] = cvtpk(e[2], e[3]);
            *(LAS u32x2*)(pwp + i * 2048) = w; gor |= w[0] | w[1]; PINV(gor);
        }
        SBAR();
    }
    if (__builtin_expect(__any((gor & 0x40004000u) != 0u), 0)) *(LAS volatile unsigned*)(uintptr_t)(ad.lds0 + MISC + fcur) = 1u;
}

struct PVState { f32x4 o[9][2], ls[3]; bf16x8 dv[2], dp[NDEF]; };
template <int DG> DI void pv_step(PVState& st, const int i, const bf16x8 a0, const bf16x8 a1, const bf16x8 pf, const bf16x8 ones) {
    st.o[i][0] = MFMA32(a0, pf, st.o[i][0]);
    st.o[i][1] = MFMA32(a1, pf, st.o[i][1]);
    if ((i & 3) == DG && i < 8) st.ls[i >> 2] = MFMA32(ones, pf, st.ls[i >> 2]);
    if (i == 8 && DG == 0) st.ls[2] = MFMA32(ones, pf, st.ls[2]);
}
template <int DG> DI void pv_flush(PVState& st) {
    const u32x4 onesw = {0x3F803F80u, 0x3F803F80u, 0x3F803F80u, 0x3F803F80u};
    const bf16x8 ones = __builtin_bit_cast(bf16x8, onesw);
#pragma unroll
    for (int d = 0; d < NDEF; ++d) { pv_step<DG>(st, 9 - NDEF + d, st.dv[0], st.dv[1], st.dp[d], ones); SBAR(); }
}
template <int DG, typename F> DI void pv_iter(PVState& st, const AAddr& ad, const unsigned vo, const unsigned pcur, F&& dma) {
    const unsigned pprev = PSZ - pcur, fprev = (pprev ? 4u : 0u);
    s16x4 vf[2][2][2];
    bf16x8 pf[18];
    const unsigned flagv = *(LAS volatile unsigned*)(uintptr_t)(ad.lds0 + MISC + fprev);
    const lptr v0p = LP(ad.va0 + vo), v1p = LP(ad.va1 + vo);
    const lptr prp = LP(ad.pr + pprev);
    vf[0][0][0] = trread(v0p); vf[0][0][1] = trread(v0p + 4096);
    vf[0][1][0] = trread(v1p); vf[0][1][1] = trread(v1p + 4096);
#pragma unroll
    for (int k = 0; k < 4; ++k) pf[k] = ldsread16(prp + k * 2048);
    dma(0);
    SBAR();
    pv_flush<DG>(st);
    if (__builtin_expect(__builtin_amdgcn_readfirstlane(flagv) != 0, 0)) {
        BAR();
#pragma unroll
        for (int i = 0; i < 9; ++i) {
            const unsigned a = ad.lds0 + RMX + (i * 16 + ad.r16) * 4;
            const float m0 = *(LAS float*)(uintptr_t)a, m1 = *(LAS float*)(uintptr_t)(a + 576), m2 = *(LAS float*)(uintptr_t)(a + 1152), m3 = *(LAS float*)(uintptr_t)(a + 1728);
            const float dl = __builtin_fmaxf(__builtin_fmaxf(fmax3(m0, m1, m2), m3) + PSH, 0.f);
            const float al = __builtin_amdgcn_exp2f(-dl);
            st.o[i][0] *= al; st.o[i][1] *= al;
            if ((i & 3) == DG && i < 8) st.ls[i >> 2] *= al;
            if (i == 8 && DG == 0) st.ls[2] *= al;
        }
        BAR();
#pragma unroll
        for (int k = 0; k < 4; ++k) pf[k] = ldsread16(prp + k * 2048);
    }
    const u32x4 onesw = {0x3F803F80u, 0x3F803F80u, 0x3F803F80u, 0x3F803F80u};
    const bf16x8 ones = __builtin_bit_cast(bf16x8, onesw);
    SBAR();
#pragma unroll
    for (int k = 0; k < 18 - NDEF; ++k) {
        const int u = k / 9, i = k % 9;
        if (k + 4 < 18) pf[k + 4] = ldsread16(prp + (((k + 4) % 9) * 2048 + ((k + 4) / 9) * 1024));
        if (k == 2) { vf[1][0][0] = trread(v0p + 8192); vf[1][0][1] = trread(v0p + 12288); }
        if (k == 3) { vf[1][1][0] = trread(v1p + 8192); vf[1][1][1] = trread(v1p + 12288); }
        pv_step<DG>(st, i, cat(vf[u][0][0], vf[u][0][1]), cat(vf[u][1][0], vf[u][1][1]), pf[k], ones);
        if (k == 2) dma(1);
        SBAR();
    }
    st.dv[0] = cat(vf[1][0][0], vf[1][0][1]); st.dv[1] = cat(vf[1][1][0], vf[1][1][1]);
#pragma unroll
    for (int d = 0; d < NDEF; ++d) st.dp[d] = pf[18 - NDEF + d];
}

__global__ __launch_bounds__(512, 2) void attn_k(const __hip_bfloat16* __restrict__ x, const __hip_bfloat16* __restrict__ kc,
                                                 const __hip_bfloat16* __restrict__ vc, const __hip_bfloat16* __restrict__ wq,
                                                 const __hip_bfloat16* __restrict__ wo, __hip_bfloat16* __restrict__ out,
                                                 char* __restrict__ ws) {
    extern __shared__ __attribute__((aligned(16))) char lds[];
    const int tid = threadIdx.x, lane = tid & 63;
    const int wid = __builtin_amdgcn_readfirstlane(tid >> 6);
    const int bid = blockIdx.x, xcd = bid & 7, idx = bid >> 3;
    const int nt = idx & 3, h = 4 * xcd + nt, b = idx >> 2;
    const unsigned lds0 = (unsigned)(uintptr_t)lds;
    const unsigned r16 = lane & 15, g = lane >> 4, qq = r16 >> 2, p = lane & 3;
    const int w4 = wid & 3;
    {
        unsigned long long pc; unsigned sv;
        asm volatile("s_getpc_b64 %0" : "=s"(pc));
        const unsigned blk = (unsigned)(idx * 8 + wid) * 8192u;
        if (blk + 8192u <= CODEWARM)
            asm volatile("s_mov_b32 %0, m0\n\ts_mov_b32 m0, %3\n\ts_nop 0\n\tglobal_load_lds_dword %1, %2\n\ts_mov_b32 m0, %0"
                         : "=&s"(sv) : "v"(blk + lane * 128u), "s"(pc), "s"(lds0 + MISC + 6144) : "memory");
    }
    AAddr ad;
    ad.lds0 = lds0; ad.r16 = r16; ad.g = g;
    {
        const unsigned s0 = qq | (((g ^ (g >> 1)) & 1) << 2), vrow = lds0 + 256 * (4 * g + qq) + 8 * p;
        ad.va0 = vrow + 32 * ((2 * w4) ^ s0); ad.va1 = vrow + 32 * ((2 * w4 + 1) ^ s0);
    }
#define XSL(c) ((((c) + 1) % 3) == 0 ? 2 : (((c) + 1) % 3) == 1 ? 5 : 4)
#define WSL(c) ((((c) + 1) % 3) == 0 ? 3 : (((c) + 1) % 3) == 1 ? 6 : 8)
#define QXO(qt) ((qt) < 8 ? 5 * SLOT + (qt) * 4096 : MISC + 8192)
    unsigned long long* f1 = (unsigned long long*)(ws + WS_F1 + (b * 8 + xcd) * 512);
    const char* slabs = ws + WS_P + (size_t)(b * 4 + nt) * 8 * 8 * 5120;
    unsigned long long* f2 = (unsigned long long*)(ws + WS_F2 + (b * 4 + nt) * 8 * 64);
    unsigned ro = 2 * SLOT;
    unsigned pcur = PSZ;

    if (wid < 4) {
        asm volatile("; compute waves" ::: "memory");
        if (tid < 8 && tid != 2) *(LAS unsigned*)(uintptr_t)(lds0 + MISC + tid * 4) = 0u;
#pragma unroll
        for (int s = 0; s < 4; ++s) ad.kad[s] = lds0 + 4096 * w4 + 256 * r16 + 16 * ((4 * s + g) ^ r16);
        ad.pr = 0; ad.pw = lds0 + PB + (w4 >> 1) * 1024 + lane * 16 + (w4 & 1) * 8;
        {
            const unsigned s0w = qq | ((g & 1) << 2);
            const unsigned wrow = lds0 + 256 * (8 * g + qq) + 8 * p;
            const unsigned wa0 = wrow + 32 * ((2 * w4) ^ s0w), wa1 = wrow + 32 * ((2 * w4 + 1) ^ s0w);
            const unsigned xad = lds0 + 128 * r16 + 16 * (g ^ ((r16 >> 1) & 7));
            unsigned xead[4];
#pragma unroll
            for (int s = 0; s < 4; ++s) xead[s] = lds0 + 7 * SLOT + 1024 * r16 + 16 * ((4 * s + g) ^ r16);
            f32x4 acc[9][2];
#pragma unroll
            for (int i = 0; i < 9; ++i) { acc[i][0] = f32x4{0.f, 0.f, 0.f, 0.f}; acc[i][1] = f32x4{0.f, 0.f, 0.f, 0.f}; }
            s16x4 al[2][2], ah[2][2]; bf16x8 xb_[2][9];
#define RDA(bu, s, j) do { const int o_ = WSL((s) >> 1) * SLOT + ((s) & 1) * 8192; const unsigned a_ = (j) ? wa1 : wa0; \
            al[bu][j] = trread(LP(a_) + o_); ah[bu][j] = trread(LP(a_ ^ 128) + (o_ + 1024)); } while (0)
#define RDB(bu, s, i) do { if ((i) < 8) xb_[bu][i] = ldsread16(LP(xad ^ (((s) & 1) * 64)) + (XSL((s) >> 1) * SLOT + (i) * 2048)); \
            else xb_[bu][i] = ldsread16(LP(xead[(s) & 3]) + ((s) >> 2) * 256); } while (0)
            BAR();
            RDA(0, 0, 0); RDA(0, 0, 1);
#pragma unroll
            for (int i = 0; i < 9; ++i) RDB(0, 0, i);
#pragma unroll
            for (int s = 0; s < 16; ++s) {
                const int bu = s & 1;
                if ((s & 1) && s < 15) BAR();
                SBAR();
#pragma unroll
                for (int i = 0; i < 9; ++i) {
                    acc[i][0] = MFMA32(cat(al[bu][0], ah[bu][0]), xb_[bu][i], acc[i][0]);
                    if (s < 15) { if (i < 2) RDA(bu ^ 1, s + 1, i); }
                    SBAR();
                    acc[i][1] = MFMA32(cat(al[bu][1], ah[bu][1]), xb_[bu][i], acc[i][1]);
                    if (s < 15) RDB(bu ^ 1, s + 1, i);
                    SBAR();
                }
            }
#pragma unroll
            for (int i = 0; i < 9; ++i)
                *(LAS bf16x8*)(LP(lds0 + w4 * 1024 + lane * 16) + QXO(i)) = pack_natural(acc[i][0], acc[i][1], QSCALE);
            BAR();
        }
        bf16x8 af[16][2];
#define RDAF(k, so) do { af[k][0] = cat(trread(LP(ad.va0) + (so)), trread(LP(ad.va0) + ((so) + 4096))); af[k][1] = cat(trread(LP(ad.va1) + (so)), trread(LP(ad.va1) + ((so) + 4096))); } while (0)
        {
            const char* kg = (const char*)kc + ((size_t)(b * NH + h) * SK + 16 * w4) * 256;
            QKState st;
#pragma unroll
            for (int s = 0; s < 4; ++s) { st.kf[0][s] = ldsread16(LP(ad.kad[s])); st.kf[1][s] = ldsread16(LP(lds0 + g * 16) + s * 64); }
#pragma unroll
            for (int s = 0; s < 4; ++s) {
#pragma unroll
                for (int i = 0; i < 9; ++i) st.qf[i][s] = ldsread16(LP(lds0 + lane * 16) + (QXO(i) + s * 1024));
            }
            {
                const f32x4 ps = {PSH, PSH, PSH, PSH};
#pragma unroll
                for (int s = 0; s < 4; ++s) {
#pragma unroll
                    for (int i = 0; i < 9; ++i) st.negm[i] = MFMA32(st.kf[1][s], st.qf[i][s], s == 0 ? ps : st.negm[i]);
                }
#pragma unroll
                for (int i = 0; i < 9; ++i) st.negm[i] = -st.negm[i];
            }
            BAR();
            qk_iter<0>(st, ad, 0, kg, SLOT, 0u, w4);
            __builtin_amdgcn_s_setprio(1);
            const char* kbase = kg;
            const unsigned ok0 = g * 256 + (r16 ^ g) * 16;
            auto kdma = [&](const int it) {
                if (it >= 2 && it <= 12) dma4_k(kbase + (size_t)(it + 3) * SLOT, lds0 + w4 * 4096 + ringnext(ro, 5), ok0);
            };
            for (int it = 1; it < 16; it += 2) {
                WAITV(4);
                BAR();
                qk_iter<1>(st, ad, it, kg, ringnext(ro, 1), pcur, w4);
                kdma(it);
                ro = ringnext(ro, 2); pcur = PSZ - pcur;
                if (it < 13) { WAITV(4); } else { WAITV(0); }
                BAR();
                if (it < 15) { qk_iter<0>(st, ad, it + 1, kg, ringnext(ro, 1), pcur, w4); kdma(it + 1); ro = ringnext(ro, 2); pcur = PSZ - pcur; }
            }
            {
                const unsigned pprev = PSZ - pcur, fprev = (pprev ? 4u : 0u);
                const unsigned flagv = *(LAS volatile unsigned*)(uintptr_t)(lds0 + MISC + fprev);
                if (__builtin_expect(__builtin_amdgcn_readfirstlane(flagv) != 0, 0)) fixup_qk(st, ad, kg + (size_t)15 * SLOT, pprev, fprev, w4);
            }
            __builtin_amdgcn_s_setprio(0);
        }
#pragma unroll
        for (int k = 0; k < 6; ++k) RDAF(k, ((4 + (k >> 1)) % 7) * SLOT + (k & 1) * 8192);
        BAR();
#pragma unroll
        for (int k = 6; k < 12; ++k) RDAF(k, ((4 + (k >> 1)) % 7) * SLOT + (k & 1) * 8192);
        const unsigned ticket = *(LAS volatile unsigned*)(uintptr_t)(lds0 + MISC + 8);
        BAR();
        f32x4 acc[9][2];
#pragma unroll
        for (int i = 0; i < 9; ++i) { acc[i][0] = f32x4{0.f, 0.f, 0.f, 0.f}; acc[i][1] = f32x4{0.f, 0.f, 0.f, 0.f}; }
        const unsigned bsrc = lds0 + lane * 16;
        auto pos = [&](auto pc, const unsigned bo) {
            constexpr int P = decltype(pc)::value; constexpr bool LAST = P == 3;
            bf16x8 bf[36];
            const lptr bp = LP(bsrc + bo);
#define BI(n) (LAST ? (n) / 4 : (n) % 9)
#define BS(n) (LAST ? (n) % 4 : (n) / 9)
#pragma unroll
            for (int n = 0; n < 4; ++n) bf[n] = ldsread16(bp + (BI(n) * 4096 + BS(n) * 1024));
            SBAR();
#pragma unroll
            for (int n = 0; n < 36; ++n) {
                const int i = BI(n), sp = BS(n);
                acc[i][0] = MFMA32(af[4 * P + sp][0], bf[n], acc[i][0]);
                if (n + 4 < 36) bf[n + 4] = ldsread16(bp + (BI(n + 4) * 4096 + BS(n + 4) * 1024));
                SBAR();
                acc[i][1] = MFMA32(af[4 * P + sp][1], bf[n], acc[i][1]);
                if (LAST && i > 0 && sp == 2) {
                    const int t = i - 1;
                    store16_sc1<0>(slabs + (size_t)(((t < 5 ? w4 : w4 + 4) * 8 + xcd) * 5 + (t < 5 ? t : t - 5)) * 1024, lane * 16, __builtin_bit_cast(u32x4, pack_perm(acc[t][0], acc[t][1], 1.0f)));
                }
                SBAR();
            }
        };
        pos(std::integral_constant<int, 0>{}, PB);
        BAR();
        pos(std::integral_constant<int, 1>{}, 0u);
        BAR();
        pos(std::integral_constant<int, 2>{}, 36864u);
        BAR();
#pragma unroll
        for (int k = 12; k < 16; ++k) RDAF(k, (7 + ((k - 12) >> 1)) * SLOT + (k & 1) * 8192);
        pos(std::integral_constant<int, 3>{}, 73728u);
        store16_sc1<0>(slabs + (size_t)(((w4 + 4) * 8 + xcd) * 5 + 3) * 1024, lane * 16, __builtin_bit_cast(u32x4, pack_perm(acc[8][0], acc[8][1], 1.0f)));
        WAITV(0);
        if (lane < 2) __hip_atomic_store((gu64*)(f2 + (w4 + 4 * lane) * 8 + xcd), mkflag(ticket), __ATOMIC_RELAXED, __HIP_MEMORY_SCOPE_AGENT);
        if (wid == 0 && xcd < 4) {
            for (int spin = 0; spin < (1 << 24); ++spin) { if (__builtin_amdgcn_readfirstlane(*(LAS volatile unsigned*)(uintptr_t)(lds0 + MISC + 20))) break; __builtin_amdgcn_s_sleep(1); }
            reduce_tile(slabs + (size_t)xcd * 8 * 5120 + 4 * 1024, (char*)out + ((size_t)(b * SQ + 64 + r16) * DM + nt * 128 + (xcd & 3) * 32 + g * 8) * 2, lane);
        }
    } else {
        asm volatile("; memory waves" ::: "memory");
        unsigned tick = 0;
        if (wid == 4 && lane == 0) {
            const unsigned one = 1u, zo = 0u;
            asm volatile("global_atomic_add %0, %1, %2, %3 sc0" : "=v"(tick) : "v"(zo), "v"(one), "s"(ws + WS_CTR + b * 256) : "memory");
        }
        const int rot = (b + 2 * nt) & 7;
        const char* xb = (const char*)x + (size_t)b * SQ * DM * 2;
        const char* kbase = (const char*)kc + ((size_t)(b * NH + h) * SK + 16 * w4) * 256;
        const char* vbase = (const char*)vc + ((size_t)(b * NH + h) * SK + 16 * w4) * 256;
        const char* obase = (const char*)wo + (size_t)(512 * xcd + 16 * w4) * 1024 + nt * 256;
        const char* wbase = (const char*)wq + (size_t)(16 * w4) * 8192 + h * 256;
        const char* xbase = xb + (size_t)(32 * w4) * 1024;
        const unsigned ldsq = lds0 + w4 * 4096;
        unsigned offK[4], offV[4], offO[4], offW[4], offX[4], offE[4];
#pragma unroll
        for (int j = 0; j < 4; ++j) {
            const unsigned rl = 4 * j + g, chV = r16 ^ (sw3(rl) << 1);
            offK[j] = g * 256 + (r16 ^ rl) * 16; offV[j] = g * 256 + chV * 16;
            offO[j] = rl * 1024 + chV * 16 - 1024 * j; offW[j] = rl * 8192 + chV * 16 - 1024 * j;
            const unsigned xr = 8 * j + (lane >> 3);
            offX[j] = xr * 1024 + (((lane & 7) ^ ((xr >> 1) & 7)) * 16) - 1024 * j;
            offE[j] = (((lane ^ (4 * w4 + j)) + 8 * rot) & 63) * 16;
        }
        auto issueXW = [&](int c) { const int cr = (c + rot) & 7;
                                    dma4(xbase + cr * 128, ldsq + XSL(c) * SLOT, offX[0], offX[1], offX[2], offX[3]);
                                    dma4(wbase + (size_t)cr * 64 * 8192, ldsq + WSL(c) * SLOT, offW[0], offW[1], offW[2], offW[3]); };
        auto issueK = [&](int t, unsigned so) { dma4_nt(kbase + (size_t)t * SLOT, ldsq + so, offK[0], offK[1], offK[2], offK[3]); };
        auto issueV = [&](int t, unsigned so) { dma4_nt(vbase + (size_t)t * SLOT, ldsq + so, offV[0], offV[1], offV[2], offV[3]); };
        auto issueO = [&](int c, unsigned so) { dma4(obase + (size_t)((2 * nt + c) & 7) * 64 * 1024, ldsq + so, offO[0], offO[1], offO[2], offO[3]); };
        issueXW(0);
        dma4(xb + (size_t)(128 + 4 * w4) * 1024, ldsq + 7 * SLOT, offE[0], offE[1], offE[2], offE[3]);
        issueXW(1);
        if (wid == 7) {
            const char* kb0 = (const char*)kc + (size_t)(b * NH + h) * SK * 256; const char* vb0 = (const char*)vc + (size_t)(b * NH + h) * SK * 256;
            const char* ob0 = (const char*)wo + (size_t)(512 * xcd) * 1024 + nt * 256;
            const char* src = lane < 16 ? kb0 + lane * 16384 : lane < 32 ? vb0 + (lane - 16) * 16384 : ob0 + (lane - 32) * 16384;
            unsigned sv;
            asm volatile("s_mov_b32 %0, m0\n\ts_mov_b32 m0, %2\n\ts_nop 0\n\tglobal_load_lds_dword %1, off nt\n\ts_mov_b32 m0, %0"
                         : "=&s"(sv) : "v"(src), "s"(lds0 + MISC + 6400) : "memory");
        }
        WAITV(8);
        asm volatile("" : "+v"(tick));
        if (tid == 256) *(LAS unsigned*)(uintptr_t)(lds0 + MISC + 8) = tick;
        BAR();
        issueXW(2);                   WAITV(8);  BAR();
        issueXW(3);                   WAITV(8);  BAR();
        issueXW(4);                   WAITV(8);  BAR();
        issueXW(5);                   WAITV(8);  BAR();
        issueXW(6);                   WAITV(8);  BAR();
        issueXW(7); issueK(0, 0);     WAITV(12); BAR();
        issueK(1, SLOT); issueV(0, 2 * SLOT); WAITV(12); BAR();
        issueK(2, 3 * SLOT);          WAITV(8);
        BAR();
        issueV(1, 4 * SLOT);
        ad.pr = lds0 + PB + lane * 16; ad.pw = 0;
        PVState st;
        const f32x4 z = {0.f, 0.f, 0.f, 0.f};
#pragma unroll
        for (int i = 0; i < 9; ++i) { st.o[i][0] = z; st.o[i][1] = z; }
        st.ls[0] = z; st.ls[1] = z; st.ls[2] = z;
        { const bf16x8 zb = {0, 0, 0, 0, 0, 0, 0, 0}; st.dv[0] = zb; st.dv[1] = zb;
#pragma unroll
          for (int d = 0; d < NDEF; ++d) st.dp[d] = zb; }
        BAR();
        issueK(3, 5 * SLOT); issueV(2, 6 * SLOT); issueK(4, 0);
        auto pvloop = [&](auto dgc) {
            constexpr int DG = decltype(dgc)::value;
            asm volatile("; pv %0" :: "i"(DG) : "memory");
#pragma nounroll
            for (int it = 1; it <= 16; ++it) {
                if (it == 1 || it == 15) { WAITV(16); } else if (it == 2 || it == 14) { WAITV(12); } else { WAITV(8); }
                BAR();
                const unsigned vo = it == 16 ? ringnext(ro, 6) : ro;
                pv_iter<DG>(st, ad, vo, pcur, [&](int which) {
                    if (which == 0) { if (it == 13) issueV(15, ringnext(ro, 5)); if (it >= 14) issueO(2 * it - 27, ringnext(ro, 5)); }
                    else { if (it <= 12) issueV(it + 2, ringnext(ro, 6)); else if (it <= 15) issueO(2 * it - 26, ringnext(ro, 6)); }
                });
                ro = ringnext(ro, 2); pcur = PSZ - pcur;
            }
            pv_flush<DG>(st);
        };
        if (w4 == 0) pvloop(std::integral_constant<int, 0>{}); else if (w4 == 1) pvloop(std::integral_constant<int, 1>{});
        else if (w4 == 2) pvloop(std::integral_constant<int, 2>{}); else pvloop(std::integral_constant<int, 3>{});
        if (g == 0) {
            *(LAS float*)(uintptr_t)(lds0 + LSM + (w4 * 16 + r16) * 4) = st.ls[0][0];
            *(LAS float*)(uintptr_t)(lds0 + LSM + ((w4 + 4) * 16 + r16) * 4) = st.ls[1][0];
            if (w4 == 0) *(LAS float*)(uintptr_t)(lds0 + LSM + (128 + r16) * 4) = st.ls[2][0];
        }
        WAITV(0);
        BAR();
        const char* og = ws + WS_O + (size_t)(b * NH + 4 * xcd) * 36864;
        {
            float rl[9]; bf16x8 fr[9];
#pragma unroll
            for (int i = 0; i < 9; ++i) rl[i] = *(LAS float*)(uintptr_t)(lds0 + LSM + (i * 16 + r16) * 4);
#pragma unroll
            for (int i = 0; i < 9; ++i) {
                fr[i] = pack_perm(st.o[i][0], st.o[i][1], __builtin_amdgcn_rcpf(rl[i]));
                store16_sc1<0>(og + (size_t)nt * 36864 + (i * 4 + w4) * 1024, lane * 16, __builtin_bit_cast(u32x4, fr[i]));
            }
#pragma unroll
            for (int i = 0; i < 9; ++i) *(LAS bf16x8*)(LP(lds0 + PB + w4 * 1024 + lane * 16) + i * 4096) = fr[i];
        }
        const unsigned ticket = *(LAS volatile unsigned*)(uintptr_t)(lds0 + MISC + 8);
        BAR();
        WAITV(0);
        if (lane == 0) {
            const unsigned n = __hip_atomic_fetch_add((LAS unsigned*)(uintptr_t)(lds0 + MISC + 12), 1u, __ATOMIC_RELAXED, __HIP_MEMORY_SCOPE_WORKGROUP);
            if (n == 3u) __hip_atomic_store((gu64*)(f1 + nt * 16), mkflag(ticket), __ATOMIC_RELAXED, __HIP_MEMORY_SCOPE_AGENT);
        }
        unsigned done = 0;
        int ngrp = 0, sq1 = 0, sq2 = 0, sq3 = 0;
        auto fetch = [&](const unsigned rdy) {
#pragma unroll
            for (int pp = 1; pp < 4; ++pp) {
                const int hh = (nt + pp) & 3;
                if (((rdy >> hh) & 1u) && !((done >> pp) & 1u)) {
#pragma unroll
                    for (int r = 0; r < 9; ++r) dma1_sc1(og + (size_t)hh * 36864 + (r * 4 + w4) * 1024, lds0 + (pp - 1) * 36864 + (r * 4 + w4) * 1024, lane * 16);
                    done |= 1u << pp;
                    if (pp == 1) sq1 = ngrp; else if (pp == 2) sq2 = ngrp; else sq3 = ngrp;
                    ++ngrp;
                }
            }
        };
        auto need = [&](const int pp) {
            for (int spin = 0; spin < (1 << 22); ++spin) {
                unsigned m;
                if (wid == 4) { m = pollmask(f1, ticket, lane) | (1u << nt); if (lane == 0) *(LAS volatile unsigned*)(uintptr_t)(lds0 + MISC + 16) = m; }
                else m = __builtin_amdgcn_readfirstlane(*(LAS volatile unsigned*)(uintptr_t)(lds0 + MISC + 16));
                if (spin == (1 << 22) - 1) m = 0xFu;
                fetch(m);
                if ((done >> pp) & 1u) break;
                __builtin_amdgcn_s_sleep(1);
            }
            const int younger = ngrp - 1 - (pp == 1 ? sq1 : pp == 2 ? sq2 : sq3);
            if (younger >= 2) { WAITV(18); } else if (younger == 1) { WAITV(9); } else { WAITV(0); }
        };
        need(1);
        BAR();
        issueO(6, 7 * SLOT); issueO(7, 8 * SLOT);
        if (done != 0xEu) need(2); else { if (sq2 == 2) { WAITV(8); } else if (sq3 > sq2) { WAITV(17); } else { WAITV(8); } }
        BAR();
        if (done != 0xEu) need(3);
        WAITV(0);
        BAR();
        {
            const int q0r = (xcd >> 2) ? 5 : 0;
            if (wid == 4) {
                __builtin_amdgcn_s_sleep(20);
                pollflags(f2 + xcd * 8, 8, ticket, lane);
                if (lane == 0) *(LAS volatile unsigned*)(uintptr_t)(lds0 + MISC + 20) = 1u;
            } else {
                for (int spin = 0; spin < (1 << 24); ++spin) { if (__builtin_amdgcn_readfirstlane(*(LAS volatile unsigned*)(uintptr_t)(lds0 + MISC + 20))) break; __builtin_amdgcn_s_sleep(1); }
            }
            const char* sr = slabs + (size_t)xcd * 8 * 5120;
            reduce_tile(sr + w4 * 1024, (char*)out + ((size_t)(b * SQ + (q0r + w4) * 16 + r16) * DM + nt * 128 + (xcd & 3) * 32 + g * 8) * 2, lane);
        }
        if (tid == 256) __hip_atomic_fetch_add((gu32*)(ws + WS_CTR + b * 256), 3u, __ATOMIC_RELAXED, __HIP_MEMORY_SCOPE_AGENT);
    }
}

extern "C" void kernel_launch(void* const* d_in, const int* in_sizes, int n_in, void* d_out, int out_size, void* d_ws, size_t ws_size,
                              hipStream_t stream) {
    const __hip_bfloat16* x = (const __hip_bfloat16*)d_in[0];
    const __hip_bfloat16* kc = (const __hip_bfloat16*)d_in[1];
    const __hip_bfloat16* vc = (const __hip_bfloat16*)d_in[2];
    const __hip_bfloat16* wq = (const __hip_bfloat16*)d_in[3];
    const __hip_bfloat16* wo = (const __hip_bfloat16*)d_in[4];
    (void)hipFuncSetAttribute((const void*)attn_k, hipFuncAttributeMaxDynamicSharedMemorySize, LDS_BYTES);
    attn_k<<<256, 512, LDS_BYTES, stream>>>(x, kc, vc, wq, wo, (__hip_bfloat16*)d_out, (char*)d_ws);
}
```
